```python
import torch

def _softmax(x, axis=-1):
    return torch.softmax(x, dim=axis)
def _gelu(x):
    return torch.nn.functional.gelu(x, approximate="tanh")
def _silu(x):
    return torch.nn.functional.silu(x)
def _sigmoid(x):
    return torch.sigmoid(x)
def _layernorm(x, axis=-1, eps=1e-5):
    m = x.mean(dim=axis, keepdim=True)
    v = x.var(dim=axis, keepdim=True, unbiased=False)
    return (x - m) / torch.sqrt(v + eps)
def _scatter_add(tgt, idx, src, axis=0):
    return tgt.clone().index_add_(axis, idx, src)
def _segment_sum(src, seg, n):
    out = torch.zeros((n,) + src.shape[1:], dtype=src.dtype)
    return out.index_add_(0, seg, src)
def _pad(x, pw):
    flat = tuple(v for pair in reversed(pw) for v in pair)
    return torch.nn.functional.pad(x, flat)

kernel_name = 'interleave_layers__ssm_conv_h3__mixer_channel_mlp__prompt'

def setup_inputs():

    _w = (lambda a: (lambda t: t.to(torch.bfloat16) if t.is_floating_point() else t)(torch.as_tensor(a)))
    def _A_setup():
        B, S, D = 1, 2048, 512
        ks = 4
        return {
            "q": _w((lambda *s: torch.randn(*s, dtype=torch.bfloat16) * 0.05)(B, S, D)),
            "k": _w((lambda *s: torch.randn(*s, dtype=torch.bfloat16) * 0.4)(B, S, D)),
            "v": _w((lambda *s: torch.randn(*s, dtype=torch.bfloat16) * 0.4)(B, S, D)),
            "shift_k": _w((lambda *s: torch.randn(*s, dtype=torch.bfloat16) * 0.4)(D, ks) * 0.5),
            "ssm_k": _w((lambda *s: torch.randn(*s, dtype=torch.bfloat16) * 0.4)(D, S) * (1.0 / S ** 0.5)),
            "ssm_D": _w((lambda *s: torch.randn(*s, dtype=torch.bfloat16) * 0.4)(D)),
        }
    _A_in = _A_setup()

    def _B_setup():
        B, S, D, F = 1, 2048, 512, 1792
        return {
            "x": _w((lambda *s: torch.randn(*s, dtype=torch.bfloat16) * 0.05)(B, S, D)),
            "w1": _w((lambda *s: torch.randn(*s, dtype=torch.bfloat16) * 0.8)(D, F) * (D ** -0.5)),
            "b1": _w((lambda *s: torch.randn(*s, dtype=torch.bfloat16) * 0.8)(F)),
            "w2": _w((lambda *s: torch.randn(*s, dtype=torch.bfloat16) * 0.8)(F, D) * (F ** -0.5)),
            "b2": _w((lambda *s: torch.randn(*s, dtype=torch.bfloat16) * 0.2)(D)),
        }
    _B_in = _B_setup()

    return {
        "A_q": _A_in["q"],
        "A_k": _A_in["k"],
        "A_v": _A_in["v"],
        "A_shift_k": _A_in["shift_k"],
        "A_ssm_k": _A_in["ssm_k"],
        "A_ssm_D": _A_in["ssm_D"],
        "B_x": _B_in["x"],
        "B_w1": _B_in["w1"],
        "B_b1": _B_in["b1"],
        "B_w2": _B_in["w2"],
        "B_b2": _B_in["b2"],
    }

def reference(A_q, A_k, A_v, A_shift_k, A_ssm_k, A_ssm_D, B_x, B_w1, B_b1, B_w2, B_b2):
    def _A_ref(q, k, v, shift_k, ssm_k, ssm_D):
        B, S, D = q.shape
        ks = shift_k.shape[-1]
        k_pad = _pad(k, ((0, 0), (ks - 1, 0), (0, 0)))
        k_shift = torch.zeros_like(k)
        for t in range(ks):
            k_shift = k_shift + k_pad[:, t:t + S, :] * shift_k[:, ks - 1 - t][None, None, :]
        kv = k_shift * v
        L = 2 * S
        kv_pad = _pad(kv, ((0, 0), (0, S), (0, 0)))
        kf_pad = _pad(ssm_k, ((0, 0), (0, S)))
        kv_f = (lambda x, axes=(-2,-1): torch.fft.rfft2(x.float(), dim=axes))(kv_pad[:, None, :, :], axes=(1, 2))
        kf_f = (lambda x, axes=(-2,-1): torch.fft.rfft2(x.float(), dim=axes))(kf_pad[:, None, :], axes=(1, 2))
        kf_f = (lambda x, *d: x.permute(*d))(kf_f, 1, 2, 0)
        y_f = kv_f * kf_f[None, :, :, :]
        y = (lambda x, s, axes=(-2,-1): torch.fft.irfft2(x, s=s, dim=axes).to(torch.bfloat16))(y_f, (1, L), axes=(1, 2))[:, 0, :S, :]
        y = y + kv * ssm_D[None, None, :]
        return q * y

    def _B_ref(x, w1, b1, w2, b2):
        y = _layernorm(x, axis=-1)
        h = _gelu(torch.einsum("bsd,df->bsf", y, w1) + b1)
        return torch.einsum("bsf,fd->bsd", h, w2) + b2

    x = A_q
    x = x + _A_ref(_layernorm(x, axis=-1), A_k, A_v, A_shift_k, A_ssm_k, A_ssm_D)
    x = x + _B_ref(_layernorm(x, axis=-1), B_w1, B_b1, B_w2, B_b2)
    x = x + _A_ref(_layernorm(x, axis=-1), A_k, A_v, A_shift_k, A_ssm_k, A_ssm_D)
    x = x + _B_ref(_layernorm(x, axis=-1), B_w1, B_b1, B_w2, B_b2)
    return x
```

```cpp
#include <hip/hip_runtime.h>
#include <hip/hip_bf16.h>
#include <stdint.h>
typedef unsigned short u16;
constexpr int S=2048, D=512, F=1792;
__device__ __forceinline__ float b2f(u16 x){ return __uint_as_float(((unsigned)x)<<16); }
__device__ __forceinline__ u16 f2b(float f){ unsigned u=__float_as_uint(f); u += 0x7fff + ((u>>16)&1); return (u16)(u>>16); }
__device__ __forceinline__ float rb(float f){ return b2f(f2b(f)); }

__global__ void k_kv(const u16* k,const u16* v,const u16* sk,u16* kv){
  int i=blockIdx.x*256+threadIdx.x; int t=i/D,d=i%D;
  float acc=0.f;
  for(int j=3;j>=0;--j){ float kk=(t-j>=0)?b2f(k[(t-j)*D+d]):0.f; acc=rb(acc+rb(kk*b2f(sk[d*4+j]))); }
  kv[i]=f2b(acc*b2f(v[i]));
}
__global__ void k_conv(const u16* kv,const u16* h,const u16* Dv,u16* Y){
  int i=blockIdx.x*256+threadIdx.x; int t=i/D,d=i%D;
  float acc=0.f;
  for(int s=0;s<=t;++s) acc+=b2f(kv[s*D+d])*b2f(h[d*S+t-s]);
  float y=rb(acc); y=rb(y+rb(b2f(kv[i])*b2f(Dv[d])));
  Y[i]=f2b(y);
}
__device__ float blksum(float v,float* sm){
  for(int o=32;o>0;o>>=1) v+=__shfl_xor(v,o);
  __syncthreads();
  if((threadIdx.x&63)==0) sm[threadIdx.x>>6]=v;
  __syncthreads();
  return sm[0]+sm[1]+sm[2]+sm[3];
}
__global__ void k_row(const u16* x,const u16* Y,u16* xo,u16* A){
  __shared__ float sm[4];
  int t=blockIdx.x; int d0=threadIdx.x*2;
  float a0=b2f(x[t*D+d0]),a1=b2f(x[t*D+d0+1]);
  float m=blksum(a0+a1,sm)/D; float v=blksum((a0-m)*(a0-m)+(a1-m)*(a1-m),sm)/D;
  float r=rsqrtf(v+1e-5f);
  float y0=b2f(Y[t*D+d0]),y1=b2f(Y[t*D+d0+1]);
  a0=rb(a0+rb(rb((a0-m)*r)*y0)); a1=rb(a1+rb(rb((a1-m)*r)*y1));
  xo[t*D+d0]=f2b(a0); xo[t*D+d0+1]=f2b(a1);
  m=blksum(a0+a1,sm)/D; v=blksum((a0-m)*(a0-m)+(a1-m)*(a1-m),sm)/D;
  r=rsqrtf(v+1e-5f); float v2=v*r*r; r*=rsqrtf(v2+1e-5f);
  A[t*D+d0]=f2b((a0-m)*r); A[t*D+d0+1]=f2b((a1-m)*r);
}
__global__ void k_g1(const u16* A,const u16* w1,const u16* b1,u16* H){
  __shared__ float sa[D];
  int t=blockIdx.x;
  for(int i=threadIdx.x;i<D;i+=256) sa[i]=b2f(A[t*D+i]);
  __syncthreads();
  for(int n=threadIdx.x;n<F;n+=256){ float acc=0.f; for(int k=0;k<D;++k) acc+=sa[k]*b2f(w1[k*F+n]);
    float x=acc+b2f(b1[n]); float u=0.7978845608f*(x+0.044715f*x*x*x); H[t*F+n]=f2b(0.5f*x*(1.f+tanhf(u))); }
}
__global__ void k_g2(const u16* H,const u16* w2,const u16* b2,const u16* x,u16* xo){
  __shared__ float sa[F];
  int t=blockIdx.x;
  for(int i=threadIdx.x;i<F;i+=256) sa[i]=b2f(H[t*F+i]);
  __syncthreads();
  for(int n=threadIdx.x;n<D;n+=256){ float acc=0.f; for(int k=0;k<F;++k) acc+=sa[k]*b2f(w2[k*D+n]);
    xo[t*D+n]=f2b(b2f(x[t*D+n])+rb(rb(acc)+b2f(b2[n]))); }
}
extern "C" void kernel_launch(void* const* d_in, const int* in_sizes, int n_in,
                              void* d_out, int out_size, void* d_ws, size_t ws_size,
                              hipStream_t stream) {
  const u16 *q=(const u16*)d_in[0],*k=(const u16*)d_in[1],*v=(const u16*)d_in[2],*sk=(const u16*)d_in[3],*h=(const u16*)d_in[4],*Dv=(const u16*)d_in[5];
  const u16 *w1=(const u16*)d_in[7],*b1=(const u16*)d_in[8],*w2=(const u16*)d_in[9],*b2=(const u16*)d_in[10];
  u16* ws=(u16*)d_ws; u16 *kv=ws,*Y=kv+S*D,*x1=Y+S*D,*A=x1+S*D,*x2=A+S*D,*H=x2+S*D;
  k_kv<<<S*D/256,256,0,stream>>>(k,v,sk,kv);
  k_conv<<<S*D/256,256,0,stream>>>(kv,h,Dv,Y);
  k_row<<<S,256,0,stream>>>(q,Y,x1,A);
  k_g1<<<S,256,0,stream>>>(A,w1,b1,H);
  k_g2<<<S,256,0,stream>>>(H,w2,b2,x1,x2);
  k_row<<<S,256,0,stream>>>(x2,Y,x1,A);
  k_g1<<<S,256,0,stream>>>(A,w1,b1,H);
  k_g2<<<S,256,0,stream>>>(H,w2,b2,x1,(u16*)d_out);
}
```

```cpp
#include <hip/hip_runtime.h>
#include <stdint.h>

#define GAS __attribute__((address_space(1)))
#define LAS __attribute__((address_space(3)))
typedef unsigned v4u __attribute__((ext_vector_type(4)));
typedef unsigned v2u __attribute__((ext_vector_type(2)));
typedef float f32x4 __attribute__((ext_vector_type(4)));
typedef float f32x2 __attribute__((ext_vector_type(2)));
typedef short s16x8 __attribute__((ext_vector_type(8)));
typedef short s16x4 __attribute__((ext_vector_type(4)));
typedef __bf16 bf16x2 __attribute__((ext_vector_type(2)));
typedef unsigned long long u64;
typedef GAS u64 gu64;
typedef unsigned short u16;

#define DBG_STAMP 0

namespace {
constexpr int O_E = 0;
constexpr int O_FA = 4096;
constexpr int O_FB = O_FA + 16384;
constexpr int O_C1 = O_FB + 16384;
constexpr int O_FX = O_C1 + 32768;
constexpr int O_ST1 = O_FX + 65536;
constexpr int O_B1 = O_ST1 + 16384;
constexpr int O_CS = O_B1 + 8192;
constexpr int O_FH = O_CS + 8192;
constexpr int O_ST2 = O_FH + 262144;
constexpr int ROWB = 4224;
constexpr int O_KVR = 1 << 20;
constexpr int O_QT = O_KVR + 512 * ROWB;
constexpr int O_W1T = O_QT + 512 * ROWB;
constexpr int O_W2T = O_W1T + 1835008;
constexpr int O_X1 = O_W2T + 1835008;
constexpr int O_Y = O_X1 + 2097152;
constexpr int O_X3 = O_Y + 2097152;
constexpr int O_H0 = O_X3 + 2097152;
constexpr int O_H1 = O_H0 + 7340032;
constexpr int O_END = O_H1 + 7340032;
constexpr int O_STAMP = O_END;
constexpr int L_W1 = 0, L_SCR = 114688, L_BYTES = 163840;
constexpr int L_BC = 111 * 1024;
constexpr int L_STG_T = 0, L_STG_W = 20480, L_CSP = 40960;
constexpr int HS_STR = 4128, L_HS = L_SCR, L_UR = L_SCR + 8 * HS_STR, UR_STR = 4576;
constexpr unsigned SPIN_CAP = 1u << 16;
constexpr float EPS = 1e-5f;

#define RLX_AGENT __ATOMIC_RELAXED, __HIP_MEMORY_SCOPE_AGENT
#define DRAIN() asm volatile("s_waitcnt vmcnt(0)" ::: "memory")
#define CFENCE() asm volatile("" ::: "memory")
#define SB() __builtin_amdgcn_sched_barrier(0)
#define BAR() do { asm volatile("s_waitcnt lgkmcnt(0)" ::: "memory"); __builtin_amdgcn_s_barrier(); CFENCE(); } while (0)
#define LD128(off) __builtin_amdgcn_raw_buffer_load_b128(rs, (off), 0, 16)
#define LD64(off) __builtin_amdgcn_raw_buffer_load_b64(rs, (off), 0, 16)
#define LD32(off) __builtin_amdgcn_raw_buffer_load_b32(rs, (off), 0, 16)
#define ST128(v, off) ST128S(v, off, 0, 0)
#define ST64(v, off) __builtin_amdgcn_raw_buffer_store_b64((v), rs, (off), 0, 16)
#define ST32(v, off) __builtin_amdgcn_raw_buffer_store_b32((v), rs, (off), 0, 16)
#define LD128S(vo, so, c) __builtin_amdgcn_raw_buffer_load_b128(rs, (vo) + ((c) & 4095), (so) + ((c) & ~4095), 16)
#define LD64S(vo, so, c) __builtin_amdgcn_raw_buffer_load_b64(rs, (vo) + ((c) & 4095), (so) + ((c) & ~4095), 16)
#define LD32S(vo, so, c) __builtin_amdgcn_raw_buffer_load_b32(rs, (vo) + ((c) & 4095), (so) + ((c) & ~4095), 16)
#define ST128S(v, vo, so, c) do { const v4u d_ = (v); __builtin_amdgcn_raw_buffer_store_b128(d_, rs, (vo) + ((c) & 4095), (so) + ((c) & ~4095), 16); \
                                  asm volatile("s_nop 1" :: "v"(d_)); } while (0)
#define ST64S(v, vo, so, c) __builtin_amdgcn_raw_buffer_store_b64((v), rs, (vo) + ((c) & 4095), (so) + ((c) & ~4095), 16)

__device__ __forceinline__ float lo16(unsigned x) { return __uint_as_float(x << 16); }
__device__ __forceinline__ float hi16(unsigned x) { return __uint_as_float(x & 0xffff0000u); }
__device__ __forceinline__ unsigned pk(float a, float b) {
  f32x2 v = {a, b};
  return __builtin_bit_cast(unsigned, __builtin_convertvector(v, bf16x2));
}
__device__ __forceinline__ float rbf(float a) { return lo16(pk(a, a)); }
__device__ __forceinline__ f32x4 mfma(v4u a, v4u b, f32x4 c) {
  return __builtin_amdgcn_mfma_f32_16x16x32_bf16(__builtin_bit_cast(s16x8, a), __builtin_bit_cast(s16x8, b), c, 0, 0, 0);
}
#define MF(acc, a, b) asm volatile("v_mfma_f32_16x16x32_bf16 %0, %1, %2, %0" : "+v"(acc) : "v"(a), "v"(b))
#define MF0(acc, a, b) asm volatile("v_mfma_f32_16x16x32_bf16 %0, %1, %2, 0" : "=&v"(acc) : "v"(a), "v"(b))
#define MFA(acc, a, b) asm volatile("v_mfma_f32_16x16x32_bf16 %0, %1, %2, %0" : "+v"(acc) : "a"(a), "v"(b))
#define MFA0(acc, a, b) asm volatile("v_mfma_f32_16x16x32_bf16 %0, %1, %2, 0" : "=&v"(acc) : "a"(a), "v"(b))
#define MFMA_BEGIN() do { SB(); asm volatile("s_nop 1"); SB(); } while (0)
#define MFMA_END() do { SB(); asm volatile("s_nop 7"); SB(); } while (0)
template <int C> __device__ __forceinline__ float dppf(float v) {
  return __uint_as_float(__builtin_amdgcn_update_dpp(0u, __float_as_uint(v), C, 0xf, 0xf, true));
}
__device__ __forceinline__ float sum16x(float v) {
  auto r = __builtin_amdgcn_permlane16_swap(__float_as_uint(v), __float_as_uint(v), false, false);
  return __uint_as_float(r[0]) + __uint_as_float(r[1]);
}
__device__ __forceinline__ float sum32x(float v) {
  auto r = __builtin_amdgcn_permlane32_swap(__float_as_uint(v), __float_as_uint(v), false, false);
  return __uint_as_float(r[0]) + __uint_as_float(r[1]);
}
__device__ __forceinline__ float wave_sum(float v) {
  v += dppf<0xB1>(v);
  v += dppf<0x4E>(v);
  v += dppf<0x141>(v);
  v += dppf<0x140>(v);
  return sum32x(sum16x(v));
}
__device__ __forceinline__ void wait_flags(gu64* p, bool act, u64 want) {
  for (unsigned s = 0; s < SPIN_CAP; ++s) {
    const u64 x = act ? __hip_atomic_load(p, RLX_AGENT) : want;
    if (__all(x == want)) break;
  }
  CFENCE();
}
__device__ __forceinline__ float gelu(float x) {
  const float z = x * __builtin_fmaf(x * x, -0.10294324f, -2.3022082f);
  return x * __builtin_amdgcn_rcpf(1.0f + __builtin_amdgcn_exp2f(z));
}

#define STAMP(i) do { } while (0)

struct Ctx {
  __amdgpu_buffer_rsrc_t rs;
  GAS unsigned char* ws;
  LAS unsigned char* lds;
  u64 tag;
  int lane, w, g, rh, jc, rq, cs;
};

}

__global__ void __launch_bounds__(256, 1)
h3_mixer_kernel(const unsigned* __restrict__ q, const unsigned* __restrict__ k, const unsigned* __restrict__ v,
                const v4u* __restrict__ sk, const v4u* __restrict__ hk, const u16* __restrict__ Dv,
                const unsigned char* __restrict__ w1, const u16* __restrict__ b1, const unsigned char* __restrict__ w2,
                const v2u* __restrict__ b2, v2u* __restrict__ out, unsigned char* wsp) {
  extern __shared__ __attribute__((aligned(16))) unsigned char lds_raw[];
  LAS unsigned char* lds = (LAS unsigned char*)lds_raw;
  GAS unsigned char* ws = (GAS unsigned char*)wsp;
  const __amdgpu_buffer_rsrc_t rs = __builtin_amdgcn_make_buffer_rsrc(wsp, 0, O_END, 0x00020000);
  const int b = blockIdx.x, tid = threadIdx.x, lane = tid & 63, w = __builtin_amdgcn_readfirstlane(tid >> 6);
  const int g = b & 7, j = b >> 3;
  const int l15 = lane & 15, g4 = lane >> 4;
  const int rh = j >> 4, jc = j & 15, rq = j >> 3, cs = j & 7;
  const int p = g * 32 + j;
  const int vl16 = lane * 16, vl8 = lane * 8;
  STAMP(0);

  const int cp = tid & 31, tq = tid >> 5;
  unsigned kk[11], vv[8], qq[8];
  {
    const int r0 = 64 * j + 8 * tq, col = 32 * g + cp;
#pragma unroll
    for (int i = 0; i < 11; ++i) {
      const int r = r0 - 3 + i;
      kk[i] = k[(r < 0 ? 0 : r) * 256 + col];
    }
#pragma unroll
    for (int i = 0; i < 8; ++i) vv[i] = v[(r0 + i) * 256 + col];
#pragma unroll
    for (int i = 0; i < 8; ++i) qq[i] = q[(r0 + i) * 256 + col];
    if (r0 == 0) kk[0] = kk[1] = kk[2] = 0;
  }
  const v4u tap = sk[32 * g + cp];
  const u64 e0 = __hip_atomic_load((gu64*)(ws + O_E), RLX_AGENT);
  SB();
  v4u qrow[2], hg[2][2], wraw[4];
#pragma unroll
  for (int i = 0; i < 2; ++i) qrow[i] = ((const v4u*)q)[(8 * b + 2 * w + i) * 64 + lane];
#pragma unroll
  for (int c = 0; c < 2; ++c) {
    hg[c][0] = hk[(2 * p + c) * 256 + (tid ? tid - 1 : 0)];
    hg[c][1] = hk[(2 * p + c) * 256 + tid];
  }
  int ksg = 0;
  if (b < 112) {
#pragma unroll
    for (int i = 0; i < 4; ++i)
      wraw[i] = *(const v4u*)(w1 + (32 * (4 * w + i) + (lane >> 1)) * 3584 + b * 32 + (lane & 1) * 16);
  } else {
    const int u = b < 224 ? b - 112 : 0;
    ksg = u >> 1;
    const int wq = ksg / 14, kl = ksg - 14 * wq;
    const int rowA = kl < 8 ? 112 * (4 * wq + (kl >> 1)) + 32 * (kl & 1)
                            : (kl < 12 ? 112 * (4 * wq + kl - 8) + 64 : 112 * (4 * wq + 2 * (kl - 12)) + 96);
    const int dB = kl < 12 ? 0 : 96;
#pragma unroll
    for (int i = 0; i < 4; ++i)
      wraw[i] = *(const v4u*)(w2 + (rowA + 8 * i + (lane >> 3) + (i >= 2 ? dB : 0)) * 1024 + (u & 1) * 512 + w * 128 + (lane & 7) * 16);
  }
  const float Dc0 = lo16(Dv[2 * p]), Dc1 = lo16(Dv[2 * p + 1]);
  SB();
  const u64 tag = ((u64)__builtin_amdgcn_readfirstlane((unsigned)(e0 >> 32)) << 32 | __builtin_amdgcn_readfirstlane((unsigned)e0)) +
                  0x9E3779B97F4A7C15ull;
  {
    const f32x2 t0 = {lo16(tap.x), lo16(tap.z)}, t1 = {hi16(tap.x), hi16(tap.z)}, t2 = {lo16(tap.y), lo16(tap.w)},
                t3 = {hi16(tap.y), hi16(tap.w)};
    f32x2 kf[11], r[8];
#pragma unroll
    for (int i = 0; i < 11; ++i) kf[i] = f32x2{lo16(kk[i]), hi16(kk[i])};
#pragma unroll
    for (int u = 0; u < 8; ++u)
      r[u] = (kf[u] * t3 + kf[u + 1] * t2 + kf[u + 2] * t1 + kf[u + 3] * t0) * f32x2{lo16(vv[u]), hi16(vv[u])};
    STAMP(1);
    LAS unsigned char* st = lds + L_STG_T + (2 * cp) * 144;
    *(LAS v4u*)(st + (7 - tq) * 16) = v4u{pk(r[7].x, r[6].x), pk(r[5].x, r[4].x), pk(r[3].x, r[2].x), pk(r[1].x, r[0].x)};
    *(LAS v4u*)(st + 144 + (7 - tq) * 16) = v4u{pk(r[7].y, r[6].y), pk(r[5].y, r[4].y), pk(r[3].y, r[2].y), pk(r[1].y, r[0].y)};
    *(LAS v4u*)(st + 9216 + tq * 16) =
        v4u{__builtin_amdgcn_perm(qq[1], qq[0], 0x05040100u), __builtin_amdgcn_perm(qq[3], qq[2], 0x05040100u),
            __builtin_amdgcn_perm(qq[5], qq[4], 0x05040100u), __builtin_amdgcn_perm(qq[7], qq[6], 0x05040100u)};
    *(LAS v4u*)(st + 9216 + 144 + tq * 16) =
        v4u{__builtin_amdgcn_perm(qq[1], qq[0], 0x07060302u), __builtin_amdgcn_perm(qq[3], qq[2], 0x07060302u),
            __builtin_amdgcn_perm(qq[5], qq[4], 0x07060302u), __builtin_amdgcn_perm(qq[7], qq[6], 0x07060302u)};
    BAR();
#pragma unroll
    for (int i = 0; i < 2; ++i) {
      const int ch = (tid >> 3) + 32 * i, pos = tid & 7;
      const v4u a = *(LAS v4u*)(lds + L_STG_T + ch * 144 + pos * 16);
      const v4u c = *(LAS v4u*)(lds + L_STG_T + 9216 + ch * 144 + pos * 16);
      ST128(a, O_KVR + (64 * g + ch) * ROWB + (248 - 8 * j + pos) * 16);
      ST128(c, O_QT + (64 * g + ch) * ROWB + (8 * j + pos) * 16);
    }
    if (j == 0 && tid < 32) __hip_atomic_store((gu64*)(ws + O_C1 + (tid * 8 + g) * 128), tag << 8, RLX_AGENT);
    STAMP(2);
    DRAIN();
    BAR();
    if (tid == 0) __hip_atomic_store((gu64*)(ws + O_FA + b * 64), tag, RLX_AGENT);
    STAMP(3);
  }
  {
    float ms[2][2];
#pragma unroll
    for (int i = 0; i < 2; ++i) {
      float x[8];
#pragma unroll
      for (int d = 0; d < 4; ++d) { x[2 * d] = lo16(qrow[i][d]); x[2 * d + 1] = hi16(qrow[i][d]); }
      float s = ((x[0] + x[1]) + (x[2] + x[3])) + ((x[4] + x[5]) + (x[6] + x[7]));
      const float m = wave_sum(s) * (1.0f / 512.0f);
      float sq = 0.f;
#pragma unroll
      for (int d = 0; d < 8; ++d) sq = __builtin_fmaf(x[d] - m, x[d] - m, sq);
      const float var = wave_sum(sq) * (1.0f / 512.0f);
      ms[i][0] = m;
      ms[i][1] = __builtin_sqrtf(rbf(rbf(var) + EPS));
    }
    if (lane == 0) ST128((v4u{pk(ms[0][0], ms[0][1]), pk(ms[1][0], ms[1][1]), (unsigned)tag, (unsigned)(tag >> 32)}), O_ST1 + (w * 256 + b) * 16);
    if (b < 224) {
#pragma unroll
      for (int i = 0; i < 4; ++i) *(LAS v4u*)(lds + L_STG_W + (4 * w + i) * 1024 + lane * 16) = wraw[i];
      const int tq4 = (lane >> 2) & 3, tp = lane & 3;
      if (b < 112) {
        const int jcp = b / 7, nbp = b - 7 * jcp;
        f32x4 cacc;
        v4u ones = {0x3F803F80u, 0x3F803F80u, 0x3F803F80u, 0x3F803F80u};
      asm volatile("" : "+v"(ones));
#pragma unroll
        for (int i = 0; i < 4; ++i) {
          LAS unsigned char* a = lds + L_STG_W + (4 * w + i) * 1024 + (4 * g4 + tq4) * 32 + tp * 8;
          const v2u x0 = __builtin_bit_cast(v2u, __builtin_amdgcn_ds_read_tr16_b64_v4i16((LAS s16x4*)a));
          const v2u x1 = __builtin_bit_cast(v2u, __builtin_amdgcn_ds_read_tr16_b64_v4i16((LAS s16x4*)(a + 512)));
          const v4u f = {x0.x, x0.y, x1.x, x1.y};
          ST128(f, O_W1T + ((jcp * 16 + 4 * w + i) * 7 + nbp) * 1024 + lane * 16);
          MFMA_BEGIN();
          if (i == 0) MF0(cacc, f, ones); else MF(cacc, f, ones);
        }
        MFMA_END();
        if (l15 == 0) *(LAS f32x4*)(lds + L_CSP + w * 64 + g4 * 16) = cacc;
      } else {
        const int csp = 4 * ((b - 112) & 1) + w;
#pragma unroll
        for (int nb = 0; nb < 4; ++nb) {
          LAS unsigned char* a = lds + L_STG_W + w * 4096 + (4 * g4 + tq4) * 128 + nb * 32 + tp * 8;
          const v2u x0 = __builtin_bit_cast(v2u, __builtin_amdgcn_ds_read_tr16_b64_v4i16((LAS s16x4*)a));
          const v2u x1 = __builtin_bit_cast(v2u, __builtin_amdgcn_ds_read_tr16_b64_v4i16((LAS s16x4*)(a + 2048)));
          ST128((v4u{x0.x, x0.y, x1.x, x1.y}), O_W2T + ((csp * 56 + ksg) * 4 + nb) * 1024 + lane * 16);
        }
      }
    }
#pragma unroll
    for (int c = 0; c < 2; ++c) {
      v4u a = hg[c][0];
      const v4u d = hg[c][1];
      if (tid == 0) a = v4u{0, 0, 0, 0};
      const unsigned s0 = __builtin_amdgcn_alignbyte(a.y, a.x, 2), s1 = __builtin_amdgcn_alignbyte(a.z, a.y, 2),
                     s2 = __builtin_amdgcn_alignbyte(a.w, a.z, 2), s3 = __builtin_amdgcn_alignbyte(d.x, a.w, 2),
                     s4 = __builtin_amdgcn_alignbyte(d.y, d.x, 2);
      LAS unsigned char* o = lds + L_HS + c * 4 * HS_STR + (tid + 1) * 16;
      *(LAS v4u*)(o) = v4u{s0, s1, s2, s3};
      *(LAS v4u*)(o + HS_STR) = v4u{a.y, a.z, a.w, d.x};
      *(LAS v4u*)(o + 2 * HS_STR) = v4u{s1, s2, s3, s4};
      *(LAS v4u*)(o + 3 * HS_STR) = v4u{a.z, a.w, d.x, d.y};
      if (tid == 255) {
        const unsigned u0 = __builtin_amdgcn_alignbyte(d.y, d.x, 2), u1 = __builtin_amdgcn_alignbyte(d.z, d.y, 2),
                       u2 = __builtin_amdgcn_alignbyte(d.w, d.z, 2), u3 = d.w >> 16;
        *(LAS v4u*)(o + 16) = v4u{u0, u1, u2, u3};
        *(LAS v4u*)(o + 16 + HS_STR) = v4u{d.y, d.z, d.w, 0};
        *(LAS v4u*)(o + 16 + 2 * HS_STR) = v4u{u1, u2, u3, 0};
        *(LAS v4u*)(o + 16 + 3 * HS_STR) = v4u{d.z, d.w, 0, 0};
      }
    }
    if (tid < 8) *(LAS v4u*)(lds + L_HS + tid * HS_STR) = v4u{0, 0, 0, 0};
    if (tid >= 64 && tid < 124) {
      const int i = tid - 64, c = i >= 30;
      *(LAS v4u*)(lds + L_UR + c * UR_STR + 4096 + (i - 30 * c) * 16) = v4u{0, 0, 0, 0};
    }
    STAMP(4);
    DRAIN();
    BAR();
    STAMP(5);
  }
  if (w == 0) {
    wait_flags((gu64*)(ws + O_FA + ((lane & 31) * 8 + g) * 64), lane < 32, tag);
    STAMP(6);
    v4u u[8];
#pragma unroll
    for (int i = 0; i < 8; ++i) u[i] = LD128S(vl16, O_KVR + (2 * p + (i >> 2)) * ROWB, (i & 3) * 1024);
#pragma unroll
    for (int i = 0; i < 8; ++i) *(LAS v4u*)(lds + L_UR + (i >> 2) * UR_STR + (i & 3) * 1024 + lane * 16) = u[i];
  } else {
    if (w == 1) {
      if (b < 112 && lane < 16) {
        const LAS float* cp4 = (const LAS float*)(lds + L_CSP);
        const float s = (cp4[lane] + cp4[16 + lane]) + (cp4[32 + lane] + cp4[48 + lane]);
        ST32(__float_as_uint(s), O_CS + (16 * b + lane) * 4);
        ST32((unsigned)b1[16 * b + lane] << 16, O_B1 + (16 * b + lane) * 4);
      }
      asm volatile("s_waitcnt vmcnt(0) lgkmcnt(0)" ::: "memory");
      if (lane == 0) __hip_atomic_store((gu64*)(ws + O_FB + b * 64), tag, RLX_AGENT);
    }
    wait_flags((gu64*)(ws + O_FB + (7 * jc + (lane & 7)) * 64), lane < 7, tag);
    STAMP(6);
  }
  BAR();
  STAMP(7);
  if (w != 0) {
    const unsigned voff = lane * 16;
    for (int gi = w - 1; gi < 28; gi += 3) {
      const u64 src = (u64)(uintptr_t)wsp + O_W1T + jc * 114688 + gi * 4096;
      const unsigned dst = (unsigned)(uintptr_t)lds + L_W1 + gi * 4096;
      unsigned keep;
      asm volatile(
          "s_mov_b32 %0, m0\n\ts_mov_b32 m0, %3\n\ts_nop 0\n\t"
          "global_load_lds_dwordx4 %1, %2 sc1\n\t"
          "global_load_lds_dwordx4 %1, %2 offset:1024 sc1\n\t"
          "global_load_lds_dwordx4 %1, %2 offset:2048 sc1\n\t"
          "global_load_lds_dwordx4 %1, %2 offset:3072 sc1\n\t"
          "s_mov_b32 m0, %0"
          : "=&s"(keep) : "v"(voff), "s"(src), "s"(dst) : "memory");
    }
  }
  v4u qg[2], sg[4];
#pragma unroll
  for (int c = 0; c < 2; ++c) qg[c] = LD128(O_QT + (2 * p + c) * ROWB + tid * 16);
#pragma unroll
  for (int i = 0; i < 4; ++i) sg[i] = LD128(O_ST1 + (i * 256 + tid) * 16);
  SB();
  {
    f32x4 acc[8];
    const int ch = w >> 1, rho0 = 4 * (w & 1);
    const LAS unsigned char* pa = lds + L_HS + (ch * 4 + (lane & 3)) * HS_STR + 8 * ((l15 >> 2) + 2 * g4) + 64 * rho0;
    const LAS unsigned char* pb = lds + L_UR + ch * UR_STR + 2 * (240 - 16 * l15 + 8 * g4) + 64 * rho0;
    v4u A[2][8], B[2][8];
#pragma unroll
    for (int s = 0; s < 8; ++s) {
      const v2u a0 = *(const LAS v2u*)(pa + 512 * s), a1 = *(const LAS v2u*)(pa + 512 * s + 8);
      A[0][s] = v4u{a0.x, a0.y, a1.x, a1.y};
      B[0][s] = *(const LAS v4u*)(pb + 512 * (7 - s));
    }
    MFMA_BEGIN();
#pragma unroll
    for (int r = 0; r < 4; ++r) {
      int n = 0;
#pragma unroll
      for (int sgm = 0; sgm < 8; ++sgm) {
#pragma unroll
        for (int dl = 0; dl + sgm < 8; ++dl) {
          if (r == 0 && sgm == 0) MF0(acc[dl], A[0][0], B[0][dl]);
          else MF(acc[sgm + dl], A[r & 1][sgm], B[r & 1][dl]);
          if (r < 3 && (n & 1) == 0 && (n >> 1) < 16) {
            const int s = n >> 2;
            if ((n & 2) == 0) {
              B[(r + 1) & 1][s] = *(const LAS v4u*)(pb + 64 * (r + 1) + 512 * (7 - s));
            } else {
              const v2u a0 = *(const LAS v2u*)(pa + 64 * (r + 1) + 512 * s), a1 = *(const LAS v2u*)(pa + 64 * (r + 1) + 512 * s + 8);
              A[(r + 1) & 1][s] = v4u{a0.x, a0.y, a1.x, a1.y};
            }
          }
          SB();
          ++n;
        }
      }
    }
    MFMA_END();
    STAMP(8);
    BAR();
    LAS unsigned char* rw = lds + L_SCR + w * 8192 + (g4 & 1) * 4096 + (2 * l15 + (g4 >> 1)) * 16;
#pragma unroll
    for (int c = 0; c < 8; ++c) *(LAS f32x4*)(rw + c * 512) = acc[c];
    BAR();
  }
  {
    for (unsigned s = 0; s < SPIN_CAP; ++s) {
      bool ok = true;
#pragma unroll
      for (int i = 0; i < 4; ++i) ok &= sg[i].z == (unsigned)tag && sg[i].w == (unsigned)(tag >> 32);
      if (__all(ok)) break;
      CFENCE();
#pragma unroll
      for (int i = 0; i < 4; ++i) sg[i] = LD128(O_ST1 + (i * 256 + tid) * 16);
    }
    float mu[8], ri[8];
#pragma unroll
    for (int i = 0; i < 4; ++i) {
      mu[2 * i] = lo16(sg[i].x); ri[2 * i] = __builtin_amdgcn_rcpf(hi16(sg[i].x));
      mu[2 * i + 1] = lo16(sg[i].y); ri[2 * i + 1] = __builtin_amdgcn_rcpf(hi16(sg[i].y));
    }
    float yv[2][8], xv[2][8];
#pragma unroll
    for (int c = 0; c < 2; ++c) {
      const v4u kr = *(const LAS v4u*)(lds + L_UR + c * UR_STR + (255 - tid) * 16);
      const float Dc = c ? Dc1 : Dc0;
#pragma unroll
      for (int hf = 0; hf < 2; ++hf) {
        const f32x4 y4 = *(const LAS f32x4*)(lds + L_SCR + (2 * c) * 8192 + hf * 4096 + tid * 16) +
                         *(const LAS f32x4*)(lds + L_SCR + (2 * c + 1) * 8192 + hf * 4096 + tid * 16);
#pragma unroll
        for (int i = 0; i < 4; ++i) {
          const int u = 4 * hf + i;
          const unsigned kd = kr[(7 - u) >> 1], qd = qg[c][u >> 1];
          const float kvv = (u & 1) ? lo16(kd) : hi16(kd);
          const float qv = (u & 1) ? hi16(qd) : lo16(qd);
          const float y = rbf(__builtin_fmaf(kvv, Dc, y4[i]));
          yv[c][u] = y;
          xv[c][u] = __builtin_fmaf((qv - mu[u]) * ri[u], y, qv);
        }
      }
    }
#pragma unroll
    for (int hf = 0; hf < 2; ++hf) {
      const int o = (hf * 32 + (tid >> 3)) * 32768 + p * 128 + (tid & 7) * 16;
      ST128((v4u{pk(xv[0][4 * hf], xv[1][4 * hf]), pk(xv[0][4 * hf + 1], xv[1][4 * hf + 1]), pk(xv[0][4 * hf + 2], xv[1][4 * hf + 2]),
                 pk(xv[0][4 * hf + 3], xv[1][4 * hf + 3])}), O_X1 + o);
      ST128((v4u{pk(yv[0][4 * hf], yv[1][4 * hf]), pk(yv[0][4 * hf + 1], yv[1][4 * hf + 1]), pk(yv[0][4 * hf + 2], yv[1][4 * hf + 2]),
                 pk(yv[0][4 * hf + 3], yv[1][4 * hf + 3])}), O_Y + o);
    }
    STAMP(9);
    DRAIN();
    BAR();
    STAMP(10);
  }
  const v4u wlast = *(const LAS v4u*)(lds + L_BC + lane * 16);
  const unsigned bcv = LD32((tid < 128 ? O_B1 : O_CS) + (112 * jc + ((tid & 127) < 112 ? (tid & 127) : 0)) * 4);
  if (tid < 32) __hip_atomic_fetch_add((gu64*)(ws + O_C1 + (tid * 8 + g) * 128), 1ull, RLX_AGENT);
  v4u w2r[14][4];
  wait_flags((gu64*)(ws + O_FB + (112 + 2 * (14 * w + (lane < 14 ? lane : 0)) + (cs >> 2)) * 64), lane < 14, tag);
#pragma unroll
  for (int i = 0; i < 14; ++i)
#pragma unroll
    for (int nb = 0; nb < 4; ++nb) w2r[i][nb] = LD128S(vl16, O_W2T + (cs * 56 + 14 * w) * 4096, (i * 4 + nb) * 1024);
  SB();
  asm volatile("" :: "a"(wlast));
  BAR();
  *(LAS unsigned*)(lds + L_BC + tid * 4) = bcv;
  if (w == 0) wait_flags((gu64*)(ws + O_C1 + ((b & 31) * 8 + (lane & 7)) * 128), lane < 8, (tag << 8) + 32);
  BAR();
  if (b == 0 && tid == 0) __hip_atomic_store((gu64*)(ws + O_E), tag, RLX_AGENT);
  STAMP(11);

  const int vb = 8 * g + 4 * rh + w;
  const int vbo = 8 * g + 2 * rq + (w >> 1), eo = w & 1;
  unsigned xres[8], yres[8];
  const LAS unsigned char* wl0 = lds + vl16;
  const LAS unsigned char* wl1 = lds + vl16 + 57344;
  asm volatile("" : "+v"(wl0), "+v"(wl1));
#define W1F(f) (*(const LAS v4u*)((f) < 56 ? wl0 + (f) * 1024 : wl1 + ((f) - 56) * 1024))
  v2u b2r[4];
#pragma unroll
  for (int nb = 0; nb < 4; ++nb) b2r[nb] = b2[16 * cs + 4 * nb + g4];

#pragma unroll
  for (int L = 0; L < 2; ++L) {
    const int OX = (L ? O_X3 : O_X1) + vb * 32768;
    const int OH = L ? O_H1 : O_H0;
    v4u F[16][2];
    f32x4 a1[4][2], aS[2], aQ[2];
    float rr[2], mr[2];
    {
      constexpr int WIN = 6;
      v4u raw[16][2];
#pragma unroll
      for (int ks = 0; ks < WIN; ++ks) { raw[ks][0] = LD128S(vl16, OX, ks * 2048); raw[ks][1] = LD128S(vl16, OX, ks * 2048 + 1024); }
      v4u ones = {0x3F803F80u, 0x3F803F80u, 0x3F803F80u, 0x3F803F80u};
      asm volatile("" : "+v"(ones));
      const bool hi = lane & 8;
      v4u W[16][4];
#pragma unroll
      for (int nb = 0; nb < 4; ++nb) W[0][nb] = W1F(nb);
#define SHUF(ks, e, i) do { if (L == 0) { const unsigned a_ = raw[ks][i][e], c_ = raw[ks][i][2 + (e)]; \
        const unsigned ar_ = __builtin_amdgcn_update_dpp(0u, a_, 0x128, 0xf, 0xf, true), cr_ = __builtin_amdgcn_update_dpp(0u, c_, 0x128, 0xf, 0xf, true); \
        F[ks][e][2 * (i)] = hi ? cr_ : a_; F[ks][e][2 * (i) + 1] = hi ? c_ : ar_; } \
        else if ((i) == 0) F[ks][e] = raw[ks][e]; } while (0)
      SHUF(0, 0, 0); SHUF(0, 0, 1); SHUF(0, 1, 0); SHUF(0, 1, 1);
      MFMA_BEGIN();
#pragma unroll
      for (int ks = 0; ks < 16; ++ks) {
#pragma unroll
        for (int n = 0; n < 12; ++n) {
          const int e = n & 1, nb = n >> 1;
          if (n < 8) { if (ks == 0) MF0(a1[nb][e], W[ks][nb], F[ks][e]); else MF(a1[nb][e], W[ks][nb], F[ks][e]); }
          else if (n < 10) { if (ks == 0) MF0(aS[e], ones, F[ks][e]); else MF(aS[e], ones, F[ks][e]); }
          else { if (ks == 0) MF0(aQ[e], F[ks][e], F[ks][e]); else MF(aQ[e], F[ks][e], F[ks][e]); }
          if (ks < 15 && n < 4) W[ks + 1][n] = W1F((ks + 1) * 7 + n);
          if (ks + WIN < 16 && n < 2) raw[ks + WIN][n] = LD128S(vl16, OX, (ks + WIN) * 2048 + n * 1024);
          if (ks < 15 && n >= 3 && n < 11 && (n & 1)) SHUF(ks + 1, ((n - 3) >> 1) & 1, (n - 3) >> 2);
          SB();
        }
      }
      MFMA_END();
    }
    STAMP(12 + 10 * L);
#pragma unroll
    for (int e = 0; e < 2; ++e) {
      const float d = (lane & 2) ? ((lane & 1) ? aQ[e][3] : aQ[e][2]) : ((lane & 1) ? aQ[e][1] : aQ[e][0]);
      const float sq = __uint_as_float(__builtin_amdgcn_ds_bpermute((l15 + 16 * (l15 >> 2)) * 4, __float_as_uint(d)));
      const float mu = aS[e][0] * (1.0f / 512.0f);
      const float var = __builtin_fmaxf(__builtin_fmaf(-mu, mu, sq * (1.0f / 512.0f)), 0.f);
      const float r1 = __builtin_amdgcn_rsqf(var + EPS);
      const float r = r1 * __builtin_amdgcn_rsqf(__builtin_fmaf(var * r1, r1, EPS));
      rr[e] = r;
      mr[e] = -mu * r;
    }
    const int hks = 14 * (jc >> 2), jq = jc & 3;
#define BV(nb) (*(const LAS f32x4*)(lds + L_BC + (16 * (nb) + 4 * g4) * 4))
#define CV(nb) (*(const LAS f32x4*)(lds + L_BC + 512 + (16 * (nb) + 4 * g4) * 4))
#define GELU_PAIR(acc, nb, e, i) pk(gelu(__builtin_fmaf(acc[i], rr[e], __builtin_fmaf(mr[e], CV(nb)[i], BV(nb)[i]))), \
                                   gelu(__builtin_fmaf(acc[(i) + 1], rr[e], __builtin_fmaf(mr[e], CV(nb)[(i) + 1], BV(nb)[(i) + 1]))))
#pragma unroll
    for (int e = 0; e < 2; ++e)
#pragma unroll
      for (int i = 0; i < 2; ++i) {
        const v4u o = {GELU_PAIR(a1[2 * i][e], 2 * i, e, 0), GELU_PAIR(a1[2 * i][e], 2 * i, e, 2),
                       GELU_PAIR(a1[2 * i + 1][e], 2 * i + 1, e, 0), GELU_PAIR(a1[2 * i + 1][e], 2 * i + 1, e, 2)};
        ST128S(o, vl16, OH + (vb * 112 + hks + 2 * jq) * 1024, (e * 56 + i) * 1024);
      }
    STAMP(13 + 10 * L);
    f32x4 a2[3][2];
    {
      v4u W[16][3];
#pragma unroll
      for (int ks = 0; ks < 2; ++ks)
#pragma unroll
        for (int nb = 0; nb < 3; ++nb) W[ks][nb] = W1F(ks * 7 + 4 + nb);
      MFMA_BEGIN();
#pragma unroll
      for (int ks = 0; ks < 16; ++ks) {
#pragma unroll
        for (int n = 0; n < 6; ++n) {
          const int e = n & 1, nb = n >> 1;
          if (ks == 0) MF0(a2[nb][e], W[ks][nb], F[ks][e]);
          else if (ks == 15 && nb == 2) MFA(a2[nb][e], wlast, F[ks][e]);
          else MF(a2[nb][e], W[ks][nb], F[ks][e]);
          if (ks < 14 && e == 0 && !(ks == 13 && nb == 2)) W[ks + 2][nb] = W1F((ks + 2) * 7 + 4 + nb);
          if (ks == 11 && n == 5) {
            DRAIN();
            if (lane == 0) __hip_atomic_store((gu64*)(ws + O_FH + (((L * 2) * 64 + vb) * 16 + jc) * 64), tag, RLX_AGENT);
          }
          SB();
        }
      }
      MFMA_END();
    }
    if (L == 0) {
      const int so = vbo * 32768 + cs * 4096 + eo * 4, vo = g4 * 256 + (l15 & 7) * 16 + (l15 >> 3) * 8;
#pragma unroll
      for (int i = 0; i < 8; ++i) {
        xres[i] = LD32S(vo, O_X1 + so, (i >> 1) * 1024 + (i & 1) * 128);
        yres[i] = LD32S(vo, O_Y + so, (i >> 1) * 1024 + (i & 1) * 128);
      }
    }
    STAMP(14 + 10 * L);
#pragma unroll
    for (int e = 0; e < 2; ++e) {
      const v4u o = {GELU_PAIR(a2[0][e], 4, e, 0), GELU_PAIR(a2[0][e], 4, e, 2), GELU_PAIR(a2[1][e], 5, e, 0), GELU_PAIR(a2[1][e], 5, e, 2)};
      ST128S(o, vl16, OH + (vb * 112 + hks + 8 + jq) * 1024, e * 56 * 1024);
      const v2u o2 = {GELU_PAIR(a2[2][e], 6, e, 0), GELU_PAIR(a2[2][e], 6, e, 2)};
      ST64S(o2, vl8, OH + (vb * 112 + hks + 12 + (jq >> 1)) * 1024 + (jq & 1) * 512, e * 56 * 1024);
    }
    STAMP(15 + 10 * L);
    gu64* fh = (gu64*)(ws + O_FH + (((L * 2) * 64 + 8 * g + 2 * rq + ((lane >> 2) & 1)) * 16 + 4 * w + (lane & 3)) * 64);
    wait_flags(fh, lane < 8, tag);
    DRAIN();
    if (lane == 0) __hip_atomic_store((gu64*)(ws + O_FH + (((L * 2 + 1) * 64 + vb) * 16 + jc) * 64), tag, RLX_AGENT);
    STAMP(16 + 10 * L);
    f32x4 acc[4][4];
    {
      int hb[4];
#pragma unroll
      for (int u = 0; u < 4; ++u) hb[u] = OH + ((16 * g + 4 * rq + ((w + u) & 3)) * 56 + 14 * w) * 1024;
      constexpr int WIN = 3;
      v4u hf[14][4];
#define LOAD_H(ks, u) do { if ((ks) < 12) hf[ks][u] = LD128S(vl16, hb[u], (ks) * 1024); \
        else { const v2u x0 = LD64S(vl8, hb[u], (ks) * 1024), x1 = LD64S(vl8, hb[u], (ks) * 1024 + 512); hf[ks][u] = v4u{x0.x, x0.y, x1.x, x1.y}; } } while (0)
#pragma unroll
      for (int ks = 0; ks < WIN; ++ks)
#pragma unroll
        for (int u = 0; u < 4; ++u) LOAD_H(ks, u);
      MFMA_BEGIN();
#pragma unroll
      for (int ks = 0; ks < 14; ++ks) {
        if (ks + WIN == 8) { wait_flags(fh + 64 * 16 * 8, lane < 8, tag); SB(); }
#pragma unroll
        for (int n = 0; n < 16; ++n) {
          const int u = n >> 2, nb = n & 3;
          if (ks == 0) MFA0(acc[u][nb], w2r[ks][nb], hf[ks][u]); else MFA(acc[u][nb], w2r[ks][nb], hf[ks][u]);
          if (ks + WIN < 14 && nb == 0) LOAD_H(ks + WIN, u);
          SB();
        }
      }
      MFMA_END();
    }
    STAMP(17 + 10 * L);
#pragma unroll
    for (int u = 1; u < 4; ++u)
#pragma unroll
      for (int nb = 0; nb < 4; ++nb)
        *(LAS f32x4*)(lds + L_SCR + (((w + u) & 3) * 3 + (u - 1)) * 4096 + nb * 1024 + lane * 16) = acc[u][nb];
    BAR();
    float xo[16];
#pragma unroll
    for (int nb = 0; nb < 4; ++nb) {
      f32x4 s = acc[0][nb];
#pragma unroll
      for (int u = 0; u < 3; ++u) s += *(const LAS f32x4*)(lds + L_SCR + (w * 3 + u) * 4096 + nb * 1024 + lane * 16);
      xo[4 * nb + 0] = s[0] + lo16(b2r[nb].x) + lo16(xres[2 * nb]);
      xo[4 * nb + 1] = s[1] + hi16(b2r[nb].x) + hi16(xres[2 * nb]);
      xo[4 * nb + 2] = s[2] + lo16(b2r[nb].y) + lo16(xres[2 * nb + 1]);
      xo[4 * nb + 3] = s[3] + hi16(b2r[nb].y) + hi16(xres[2 * nb + 1]);
    }
    STAMP(18 + 10 * L);
    if (L == 0) {
      float s = 0.f;
#pragma unroll
      for (int i = 0; i < 8; ++i) {
        const unsigned t = pk(xo[2 * i], xo[2 * i + 1]);
        xo[2 * i] = lo16(t);
        xo[2 * i + 1] = hi16(t);
        s += xo[2 * i] + xo[2 * i + 1];
      }
      const float mb = sum32x(sum16x(s)) * (1.0f / 64.0f);
      float m2 = 0.f;
#pragma unroll
      for (int i = 0; i < 16; ++i) m2 = __builtin_fmaf(xo[i] - mb, xo[i] - mb, m2);
      m2 = sum32x(sum16x(m2));
      const int so = O_ST2 + (vbo * 2 + eo) * 2048;
      if (lane < 16) ST128S((v4u{__float_as_uint(mb), __float_as_uint(m2), (unsigned)tag, (unsigned)(tag >> 32)}), vl16, so + cs * 256, 0);
      v4u s0, s1;
      for (unsigned it = 0; it < SPIN_CAP; ++it) {
        CFENCE();
        s0 = LD128S(vl16, so, 0);
        s1 = LD128S(vl16, so, 1024);
        if (__all(s0.z == (unsigned)tag && s0.w == (unsigned)(tag >> 32) && s1.z == (unsigned)tag && s1.w == (unsigned)(tag >> 32))) break;
      }
      STAMP(19);
      const float ma = __uint_as_float(s0.x), mc = __uint_as_float(s1.x);
      const float mu = sum32x(sum16x(ma + mc)) * 0.125f;
      const float da = ma - mu, dc = mc - mu;
      const float tot = sum32x(sum16x(__uint_as_float(s0.y) + __uint_as_float(s1.y) + 64.0f * (da * da + dc * dc)));
      const float r = __builtin_amdgcn_rsqf(tot * (1.0f / 512.0f) + EPS);
#pragma unroll
      for (int i = 0; i < 8; ++i)
        xres[i] = pk(__builtin_fmaf((xo[2 * i] - mu) * r, lo16(yres[i]), xo[2 * i]),
                     __builtin_fmaf((xo[2 * i + 1] - mu) * r, hi16(yres[i]), xo[2 * i + 1]));
#pragma unroll
      for (int i = 0; i < 2; ++i)
        ST128S((v4u{xres[4 * i], xres[4 * i + 1], xres[4 * i + 2], xres[4 * i + 3]}), vl16, O_X3 + vbo * 32768 + (4 * cs + eo) * 1024, i * 2048);
      DRAIN();
      if (lane == 0) __hip_atomic_store((gu64*)(ws + O_FX + ((vbo * 2 + eo) * 8 + cs) * 64), tag, RLX_AGENT);
      STAMP(20);
      wait_flags((gu64*)(ws + O_FX + (vb * 16 + l15) * 64), lane < 16, tag);
      STAMP(21);
    } else {
      const int t = 64 * (vbo & 31) + 8 * (l15 & 7) + 4 * (vbo >> 5) + 2 * (l15 >> 3) + eo;
#pragma unroll
      for (int nb = 0; nb < 4; ++nb)
        out[t * 128 + 16 * cs + 4 * nb + g4] = v2u{pk(xo[4 * nb], xo[4 * nb + 1]), pk(xo[4 * nb + 2], xo[4 * nb + 3])};
      STAMP(29);
    }
  }
}

extern "C" void kernel_launch(void* const* d_in, const int* in_sizes, int n_in, void* d_out, int out_size, void* d_ws,
                              size_t ws_size, hipStream_t stream) {
  (void)in_sizes; (void)n_in; (void)out_size; (void)ws_size;
  (void)hipFuncSetAttribute((const void*)h3_mixer_kernel, hipFuncAttributeMaxDynamicSharedMemorySize, L_BYTES);
  h3_mixer_kernel<<<256, 256, L_BYTES, stream>>>(
      (const unsigned*)d_in[0], (const unsigned*)d_in[1], (const unsigned*)d_in[2], (const v4u*)d_in[3], (const v4u*)d_in[4],
      (const u16*)d_in[5], (const unsigned char*)d_in[7], (const u16*)d_in[8], (const unsigned char*)d_in[9], (const v2u*)d_in[10],
      (v2u*)d_out, (unsigned char*)d_ws);
}
```

```cpp
#define TOUCH_CODE
#include <hip/hip_runtime.h>
#include <stdint.h>

#define GAS __attribute__((address_space(1)))
#define LAS __attribute__((address_space(3)))
typedef unsigned v4u __attribute__((ext_vector_type(4)));
typedef unsigned v2u __attribute__((ext_vector_type(2)));
typedef float f32x4 __attribute__((ext_vector_type(4)));
typedef float f32x2 __attribute__((ext_vector_type(2)));
typedef short s16x8 __attribute__((ext_vector_type(8)));
typedef short s16x4 __attribute__((ext_vector_type(4)));
typedef __bf16 bf16x2 __attribute__((ext_vector_type(2)));
typedef unsigned long long u64;
typedef GAS u64 gu64;
typedef unsigned short u16;

#define DBG_STAMP 0

namespace {
constexpr int O_E = 0;
constexpr int O_FA = 4096;
constexpr int O_FB = O_FA + 16384;
constexpr int O_C1 = O_FB + 16384;
constexpr int O_FX = O_C1 + 32768;
constexpr int O_ST1 = O_FX + 65536;
constexpr int O_B1 = O_ST1 + 16384;
constexpr int O_CS = O_B1 + 8192;
constexpr int O_FH = O_CS + 8192;
constexpr int O_ST2 = O_FH + 262144;
constexpr int ROWB = 4224;
constexpr int O_KVR = 1 << 20;
constexpr int O_QT = O_KVR + 512 * ROWB;
constexpr int O_W1T = O_QT + 512 * ROWB;
constexpr int O_W2T = O_W1T + 1835008;
constexpr int O_X1 = O_W2T + 1835008;
constexpr int O_Y = O_X1 + 2097152;
constexpr int O_X3 = O_Y + 2097152;
constexpr int O_H0 = O_X3 + 2097152;
constexpr int O_H1 = O_H0 + 7340032;
constexpr int O_END = O_H1 + 7340032;
constexpr int O_STAMP = O_END;
constexpr int L_W1 = 0, L_SCR = 114688, L_BYTES = 163840;
constexpr int L_BC = 111 * 1024;
constexpr int L_STG_T = 0, L_STG_W = 20480, L_CSP = 40960;
constexpr int HS_STR = 4128, L_HS = L_SCR, L_UR = L_SCR + 8 * HS_STR, UR_STR = 4576;
constexpr unsigned SPIN_CAP = 1u << 16;
constexpr float EPS = 1e-5f;

#define RLX_AGENT __ATOMIC_RELAXED, __HIP_MEMORY_SCOPE_AGENT
#define DRAIN() asm volatile("s_waitcnt vmcnt(0)" ::: "memory")
#define CFENCE() asm volatile("" ::: "memory")
#define SB() __builtin_amdgcn_sched_barrier(0)
#define BAR() do { asm volatile("s_waitcnt lgkmcnt(0)" ::: "memory"); __builtin_amdgcn_s_barrier(); CFENCE(); } while (0)
#define LD128(off) __builtin_amdgcn_raw_buffer_load_b128(rs, (off), 0, 16)
#define LD64(off) __builtin_amdgcn_raw_buffer_load_b64(rs, (off), 0, 16)
#define LD32(off) __builtin_amdgcn_raw_buffer_load_b32(rs, (off), 0, 16)
#define ST128(v, off) ST128S(v, off, 0, 0)
#define ST64(v, off) __builtin_amdgcn_raw_buffer_store_b64((v), rs, (off), 0, 16)
#define ST32(v, off) __builtin_amdgcn_raw_buffer_store_b32((v), rs, (off), 0, 16)
#define LD128S(vo, so, c) __builtin_amdgcn_raw_buffer_load_b128(rs, (vo) + ((c) & 4095), (so) + ((c) & ~4095), 16)
#define LD64S(vo, so, c) __builtin_amdgcn_raw_buffer_load_b64(rs, (vo) + ((c) & 4095), (so) + ((c) & ~4095), 16)
#define LD32S(vo, so, c) __builtin_amdgcn_raw_buffer_load_b32(rs, (vo) + ((c) & 4095), (so) + ((c) & ~4095), 16)
#define ST128S(v, vo, so, c) do { const v4u d_ = (v); __builtin_amdgcn_raw_buffer_store_b128(d_, rs, (vo) + ((c) & 4095), (so) + ((c) & ~4095), 16); \
                                  asm volatile("s_nop 1" :: "v"(d_)); } while (0)
#define ST64S(v, vo, so, c) __builtin_amdgcn_raw_buffer_store_b64((v), rs, (vo) + ((c) & 4095), (so) + ((c) & ~4095), 16)

__device__ __forceinline__ float lo16(unsigned x) { return __uint_as_float(x << 16); }
__device__ __forceinline__ float hi16(unsigned x) { return __uint_as_float(x & 0xffff0000u); }
__device__ __forceinline__ unsigned pk(float a, float b) {
  f32x2 v = {a, b};
  return __builtin_bit_cast(unsigned, __builtin_convertvector(v, bf16x2));
}
__device__ __forceinline__ float rbf(float a) { return lo16(pk(a, a)); }
__device__ __forceinline__ f32x4 mfma(v4u a, v4u b, f32x4 c) {
  return __builtin_amdgcn_mfma_f32_16x16x32_bf16(__builtin_bit_cast(s16x8, a), __builtin_bit_cast(s16x8, b), c, 0, 0, 0);
}
#define MF(acc, a, b) asm volatile("v_mfma_f32_16x16x32_bf16 %0, %1, %2, %0" : "+v"(acc) : "v"(a), "v"(b))
#define MF0(acc, a, b) asm volatile("v_mfma_f32_16x16x32_bf16 %0, %1, %2, 0" : "=&v"(acc) : "v"(a), "v"(b))
#define MFA(acc, a, b) asm volatile("v_mfma_f32_16x16x32_bf16 %0, %1, %2, %0" : "+v"(acc) : "a"(a), "v"(b))
#define MFA0(acc, a, b) asm volatile("v_mfma_f32_16x16x32_bf16 %0, %1, %2, 0" : "=&v"(acc) : "a"(a), "v"(b))
#define MFMA_BEGIN() do { SB(); asm volatile("s_nop 1"); SB(); } while (0)
#define MFMA_END() do { SB(); asm volatile("s_nop 7"); SB(); } while (0)
template <int C> __device__ __forceinline__ float dppf(float v) {
  return __uint_as_float(__builtin_amdgcn_update_dpp(0u, __float_as_uint(v), C, 0xf, 0xf, true));
}
__device__ __forceinline__ float sum16x(float v) {
  auto r = __builtin_amdgcn_permlane16_swap(__float_as_uint(v), __float_as_uint(v), false, false);
  return __uint_as_float(r[0]) + __uint_as_float(r[1]);
}
__device__ __forceinline__ float sum32x(float v) {
  auto r = __builtin_amdgcn_permlane32_swap(__float_as_uint(v), __float_as_uint(v), false, false);
  return __uint_as_float(r[0]) + __uint_as_float(r[1]);
}
__device__ __forceinline__ float wave_sum(float v) {
  v += dppf<0xB1>(v);
  v += dppf<0x4E>(v);
  v += dppf<0x141>(v);
  v += dppf<0x140>(v);
  return sum32x(sum16x(v));
}
__device__ __forceinline__ void wait_flags(gu64* p, bool act, u64 want) {
  for (unsigned s = 0; s < SPIN_CAP; ++s) {
    const u64 x = act ? __hip_atomic_load(p, RLX_AGENT) : want;
    if (__all(x == want)) break;
  }
  CFENCE();
}
__device__ __forceinline__ float gelu(float x) {
  const float z = x * __builtin_fmaf(x * x, -0.10294324f, -2.3022082f);
  return x * __builtin_amdgcn_rcpf(1.0f + __builtin_amdgcn_exp2f(z));
}

#define STAMP(i) do { } while (0)

struct Ctx {
  __amdgpu_buffer_rsrc_t rs;
  GAS unsigned char* ws;
  LAS unsigned char* lds;
  u64 tag;
  int lane, w, g, rh, jc, rq, cs;
};

}

__global__ void __launch_bounds__(256, 1)
h3_mixer_kernel(const unsigned* __restrict__ q, const unsigned* __restrict__ k, const unsigned* __restrict__ v,
                const v4u* __restrict__ sk, const v4u* __restrict__ hk, const u16* __restrict__ Dv,
                const unsigned char* __restrict__ w1, const u16* __restrict__ b1, const unsigned char* __restrict__ w2,
                const v2u* __restrict__ b2, v2u* __restrict__ out, unsigned char* wsp) {
  extern __shared__ __attribute__((aligned(16))) unsigned char lds_raw[];
  LAS unsigned char* lds = (LAS unsigned char*)lds_raw;
  GAS unsigned char* ws = (GAS unsigned char*)wsp;
  const __amdgpu_buffer_rsrc_t rs = __builtin_amdgcn_make_buffer_rsrc(wsp, 0, O_END, 0x00020000);
  const int b = blockIdx.x, tid = threadIdx.x, lane = tid & 63, w = __builtin_amdgcn_readfirstlane(tid >> 6);
  const int g = b & 7, j = b >> 3;
  const int l15 = lane & 15, g4 = lane >> 4;
  const int rh = j >> 4, jc = j & 15, rq = j >> 3, cs = j & 7;
  const int p = g * 32 + j;
  const int vl16 = lane * 16, vl8 = lane * 8;
  STAMP(0);

  const int cp = tid & 31, tq = tid >> 5;
  unsigned kk[11], vv[8], qq[8];
  {
    const int r0 = 64 * j + 8 * tq, col = 32 * g + cp;
#pragma unroll
    for (int i = 0; i < 11; ++i) {
      const int r = r0 - 3 + i;
      kk[i] = k[(r < 0 ? 0 : r) * 256 + col];
    }
#pragma unroll
    for (int i = 0; i < 8; ++i) vv[i] = v[(r0 + i) * 256 + col];
#pragma unroll
    for (int i = 0; i < 8; ++i) qq[i] = q[(r0 + i) * 256 + col];
    if (r0 == 0) kk[0] = kk[1] = kk[2] = 0;
  }
  const v4u tap = sk[32 * g + cp];
  const u64 e0 = __hip_atomic_load((gu64*)(ws + O_E), RLX_AGENT);
  unsigned touch = 0;
  {
    u64 pc;
    asm volatile("s_getpc_b64 %0" : "=s"(pc));
    const int o = j * 1408 + tid * 16;
    if (tid < 88) { const v4u c = *(const GAS v4u*)((pc & ~15ull) + o); touch += c.x ^ c.w; }
  }
  SB();
  v4u qrow[2], hg[2][2], wraw[4];
#pragma unroll
  for (int i = 0; i < 2; ++i) qrow[i] = ((const v4u*)q)[(8 * b + 2 * w + i) * 64 + lane];
#pragma unroll
  for (int c = 0; c < 2; ++c) {
    hg[c][0] = hk[(2 * p + c) * 256 + (tid ? tid - 1 : 0)];
    hg[c][1] = hk[(2 * p + c) * 256 + tid];
  }
  int ksg = 0;
  if (b < 112) {
#pragma unroll
    for (int i = 0; i < 4; ++i)
      wraw[i] = *(const v4u*)(w1 + (32 * (4 * w + i) + (lane >> 1)) * 3584 + b * 32 + (lane & 1) * 16);
  } else {
    const int u = b < 224 ? b - 112 : 0;
    ksg = u >> 1;
    const int wq = ksg / 14, kl = ksg - 14 * wq;
    const int rowA = kl < 8 ? 112 * (4 * wq + (kl >> 1)) + 32 * (kl & 1)
                            : (kl < 12 ? 112 * (4 * wq + kl - 8) + 64 : 112 * (4 * wq + 2 * (kl - 12)) + 96);
    const int dB = kl < 12 ? 0 : 96;
#pragma unroll
    for (int i = 0; i < 4; ++i)
      wraw[i] = *(const v4u*)(w2 + (rowA + 8 * i + (lane >> 3) + (i >= 2 ? dB : 0)) * 1024 + (u & 1) * 512 + w * 128 + (lane & 7) * 16);
  }
  const float Dc0 = lo16(Dv[2 * p]), Dc1 = lo16(Dv[2 * p + 1]);
  SB();
  const u64 tag = ((u64)__builtin_amdgcn_readfirstlane((unsigned)(e0 >> 32)) << 32 | __builtin_amdgcn_readfirstlane((unsigned)e0)) +
                  0x9E3779B97F4A7C15ull;
  {
    const f32x2 t0 = {lo16(tap.x), lo16(tap.z)}, t1 = {hi16(tap.x), hi16(tap.z)}, t2 = {lo16(tap.y), lo16(tap.w)},
                t3 = {hi16(tap.y), hi16(tap.w)};
    f32x2 kf[11], r[8];
#pragma unroll
    for (int i = 0; i < 11; ++i) kf[i] = f32x2{lo16(kk[i]), hi16(kk[i])};
#pragma unroll
    for (int u = 0; u < 8; ++u)
      r[u] = (kf[u] * t3 + kf[u + 1] * t2 + kf[u + 2] * t1 + kf[u + 3] * t0) * f32x2{lo16(vv[u]), hi16(vv[u])};
    STAMP(1);
    LAS unsigned char* st = lds + L_STG_T + (2 * cp) * 144;
    *(LAS v4u*)(st + (7 - tq) * 16) = v4u{pk(r[7].x, r[6].x), pk(r[5].x, r[4].x), pk(r[3].x, r[2].x), pk(r[1].x, r[0].x)};
    *(LAS v4u*)(st + 144 + (7 - tq) * 16) = v4u{pk(r[7].y, r[6].y), pk(r[5].y, r[4].y), pk(r[3].y, r[2].y), pk(r[1].y, r[0].y)};
    *(LAS v4u*)(st + 9216 + tq * 16) =
        v4u{__builtin_amdgcn_perm(qq[1], qq[0], 0x05040100u), __builtin_amdgcn_perm(qq[3], qq[2], 0x05040100u),
            __builtin_amdgcn_perm(qq[5], qq[4], 0x05040100u), __builtin_amdgcn_perm(qq[7], qq[6], 0x05040100u)};
    *(LAS v4u*)(st + 9216 + 144 + tq * 16) =
        v4u{__builtin_amdgcn_perm(qq[1], qq[0], 0x07060302u), __builtin_amdgcn_perm(qq[3], qq[2], 0x07060302u),
            __builtin_amdgcn_perm(qq[5], qq[4], 0x07060302u), __builtin_amdgcn_perm(qq[7], qq[6], 0x07060302u)};
    BAR();
#pragma unroll
    for (int i = 0; i < 2; ++i) {
      const int ch = (tid >> 3) + 32 * i, pos = tid & 7;
      const v4u a = *(LAS v4u*)(lds + L_STG_T + ch * 144 + pos * 16);
      const v4u c = *(LAS v4u*)(lds + L_STG_T + 9216 + ch * 144 + pos * 16);
      ST128(a, O_KVR + (64 * g + ch) * ROWB + (248 - 8 * j + pos) * 16);
      ST128(c, O_QT + (64 * g + ch) * ROWB + (8 * j + pos) * 16);
    }
    if (j == 0 && tid < 32) __hip_atomic_store((gu64*)(ws + O_C1 + (tid * 8 + g) * 128), tag << 8, RLX_AGENT);
    STAMP(2);
    asm volatile("" :: "v"(touch));
    DRAIN();
    BAR();
    if (tid == 0) __hip_atomic_store((gu64*)(ws + O_FA + b * 64), tag, RLX_AGENT);
    STAMP(3);
  }
  {
    float ms[2][2];
#pragma unroll
    for (int i = 0; i < 2; ++i) {
      float x[8];
#pragma unroll
      for (int d = 0; d < 4; ++d) { x[2 * d] = lo16(qrow[i][d]); x[2 * d + 1] = hi16(qrow[i][d]); }
      float s = ((x[0] + x[1]) + (x[2] + x[3])) + ((x[4] + x[5]) + (x[6] + x[7]));
      const float m = wave_sum(s) * (1.0f / 512.0f);
      float sq = 0.f;
#pragma unroll
      for (int d = 0; d < 8; ++d) sq = __builtin_fmaf(x[d] - m, x[d] - m, sq);
      const float var = wave_sum(sq) * (1.0f / 512.0f);
      ms[i][0] = m;
      ms[i][1] = __builtin_sqrtf(rbf(rbf(var) + EPS));
    }
    if (lane == 0) ST128((v4u{pk(ms[0][0], ms[0][1]), pk(ms[1][0], ms[1][1]), (unsigned)tag, (unsigned)(tag >> 32)}), O_ST1 + (w * 256 + b) * 16);
    if (b < 224) {
#pragma unroll
      for (int i = 0; i < 4; ++i) *(LAS v4u*)(lds + L_STG_W + (4 * w + i) * 1024 + lane * 16) = wraw[i];
      const int tq4 = (lane >> 2) & 3, tp = lane & 3;
      if (b < 112) {
        const int jcp = b / 7, nbp = b - 7 * jcp;
        f32x4 cacc;
        v4u ones = {0x3F803F80u, 0x3F803F80u, 0x3F803F80u, 0x3F803F80u};
      asm volatile("" : "+v"(ones));
#pragma unroll
        for (int i = 0; i < 4; ++i) {
          LAS unsigned char* a = lds + L_STG_W + (4 * w + i) * 1024 + (4 * g4 + tq4) * 32 + tp * 8;
          const v2u x0 = __builtin_bit_cast(v2u, __builtin_amdgcn_ds_read_tr16_b64_v4i16((LAS s16x4*)a));
          const v2u x1 = __builtin_bit_cast(v2u, __builtin_amdgcn_ds_read_tr16_b64_v4i16((LAS s16x4*)(a + 512)));
          const v4u f = {x0.x, x0.y, x1.x, x1.y};
          ST128(f, O_W1T + ((jcp * 16 + 4 * w + i) * 7 + nbp) * 1024 + lane * 16);
          MFMA_BEGIN();
          if (i == 0) MF0(cacc, f, ones); else MF(cacc, f, ones);
        }
        MFMA_END();
        if (l15 == 0) *(LAS f32x4*)(lds + L_CSP + w * 64 + g4 * 16) = cacc;
      } else {
        const int csp = 4 * ((b - 112) & 1) + w;
#pragma unroll
        for (int nb = 0; nb < 4; ++nb) {
          LAS unsigned char* a = lds + L_STG_W + w * 4096 + (4 * g4 + tq4) * 128 + nb * 32 + tp * 8;
          const v2u x0 = __builtin_bit_cast(v2u, __builtin_amdgcn_ds_read_tr16_b64_v4i16((LAS s16x4*)a));
          const v2u x1 = __builtin_bit_cast(v2u, __builtin_amdgcn_ds_read_tr16_b64_v4i16((LAS s16x4*)(a + 2048)));
          ST128((v4u{x0.x, x0.y, x1.x, x1.y}), O_W2T + ((csp * 56 + ksg) * 4 + nb) * 1024 + lane * 16);
        }
      }
    }
#pragma unroll
    for (int c = 0; c < 2; ++c) {
      v4u a = hg[c][0];
      const v4u d = hg[c][1];
      if (tid == 0) a = v4u{0, 0, 0, 0};
      const unsigned s0 = __builtin_amdgcn_alignbyte(a.y, a.x, 2), s1 = __builtin_amdgcn_alignbyte(a.z, a.y, 2),
                     s2 = __builtin_amdgcn_alignbyte(a.w, a.z, 2), s3 = __builtin_amdgcn_alignbyte(d.x, a.w, 2),
                     s4 = __builtin_amdgcn_alignbyte(d.y, d.x, 2);
      LAS unsigned char* o = lds + L_HS + c * 4 * HS_STR + (tid + 1) * 16;
      *(LAS v4u*)(o) = v4u{s0, s1, s2, s3};
      *(LAS v4u*)(o + HS_STR) = v4u{a.y, a.z, a.w, d.x};
      *(LAS v4u*)(o + 2 * HS_STR) = v4u{s1, s2, s3, s4};
      *(LAS v4u*)(o + 3 * HS_STR) = v4u{a.z, a.w, d.x, d.y};
      if (tid == 255) {
        const unsigned u0 = __builtin_amdgcn_alignbyte(d.y, d.x, 2), u1 = __builtin_amdgcn_alignbyte(d.z, d.y, 2),
                       u2 = __builtin_amdgcn_alignbyte(d.w, d.z, 2), u3 = d.w >> 16;
        *(LAS v4u*)(o + 16) = v4u{u0, u1, u2, u3};
        *(LAS v4u*)(o + 16 + HS_STR) = v4u{d.y, d.z, d.w, 0};
        *(LAS v4u*)(o + 16 + 2 * HS_STR) = v4u{u1, u2, u3, 0};
        *(LAS v4u*)(o + 16 + 3 * HS_STR) = v4u{d.z, d.w, 0, 0};
      }
    }
    if (tid < 8) *(LAS v4u*)(lds + L_HS + tid * HS_STR) = v4u{0, 0, 0, 0};
    if (tid >= 64 && tid < 124) {
      const int i = tid - 64, c = i >= 30;
      *(LAS v4u*)(lds + L_UR + c * UR_STR + 4096 + (i - 30 * c) * 16) = v4u{0, 0, 0, 0};
    }
    STAMP(4);
    DRAIN();
    BAR();
    STAMP(5);
  }
  if (w == 0) {
    wait_flags((gu64*)(ws + O_FA + ((lane & 31) * 8 + g) * 64), lane < 32, tag);
    STAMP(6);
    v4u u[8];
#pragma unroll
    for (int i = 0; i < 8; ++i) u[i] = LD128S(vl16, O_KVR + (2 * p + (i >> 2)) * ROWB, (i & 3) * 1024);
#pragma unroll
    for (int i = 0; i < 8; ++i) *(LAS v4u*)(lds + L_UR + (i >> 2) * UR_STR + (i & 3) * 1024 + lane * 16) = u[i];
  } else {
    if (w == 1) {
      if (b < 112 && lane < 16) {
        const LAS float* cp4 = (const LAS float*)(lds + L_CSP);
        const float s = (cp4[lane] + cp4[16 + lane]) + (cp4[32 + lane] + cp4[48 + lane]);
        ST32(__float_as_uint(s), O_CS + (16 * b + lane) * 4);
        ST32((unsigned)b1[16 * b + lane] << 16, O_B1 + (16 * b + lane) * 4);
      }
      asm volatile("s_waitcnt vmcnt(0) lgkmcnt(0)" ::: "memory");
      if (lane == 0) __hip_atomic_store((gu64*)(ws + O_FB + b * 64), tag, RLX_AGENT);
    }
    wait_flags((gu64*)(ws + O_FB + (7 * jc + (lane & 7)) * 64), lane < 7, tag);
    STAMP(6);
  }
  BAR();
  STAMP(7);
  if (w != 0) {
    const unsigned voff = lane * 16;
    for (int gi = w - 1; gi < 28; gi += 3) {
      const u64 src = (u64)(uintptr_t)wsp + O_W1T + jc * 114688 + gi * 4096;
      const unsigned dst = (unsigned)(uintptr_t)lds + L_W1 + gi * 4096;
      unsigned keep;
      asm volatile(
          "s_mov_b32 %0, m0\n\ts_mov_b32 m0, %3\n\ts_nop 0\n\t"
          "global_load_lds_dwordx4 %1, %2 sc1\n\t"
          "global_load_lds_dwordx4 %1, %2 offset:1024 sc1\n\t"
          "global_load_lds_dwordx4 %1, %2 offset:2048 sc1\n\t"
          "global_load_lds_dwordx4 %1, %2 offset:3072 sc1\n\t"
          "s_mov_b32 m0, %0"
          : "=&s"(keep) : "v"(voff), "s"(src), "s"(dst) : "memory");
    }
  }
  v4u qg[2], sg[4];
#pragma unroll
  for (int c = 0; c < 2; ++c) qg[c] = LD128(O_QT + (2 * p + c) * ROWB + tid * 16);
#pragma unroll
  for (int i = 0; i < 4; ++i) sg[i] = LD128(O_ST1 + (i * 256 + tid) * 16);
  SB();
  {
    f32x4 acc[8];
    const int ch = w >> 1, rho0 = 4 * (w & 1);
    const LAS unsigned char* pa = lds + L_HS + (ch * 4 + (lane & 3)) * HS_STR + 8 * ((l15 >> 2) + 2 * g4) + 64 * rho0;
    const LAS unsigned char* pb = lds + L_UR + ch * UR_STR + 2 * (240 - 16 * l15 + 8 * g4) + 64 * rho0;
    v4u A[2][8], B[2][8];
#pragma unroll
    for (int s = 0; s < 8; ++s) {
      const v2u a0 = *(const LAS v2u*)(pa + 512 * s), a1 = *(const LAS v2u*)(pa + 512 * s + 8);
      A[0][s] = v4u{a0.x, a0.y, a1.x, a1.y};
      B[0][s] = *(const LAS v4u*)(pb + 512 * (7 - s));
    }
    MFMA_BEGIN();
#pragma unroll
    for (int r = 0; r < 4; ++r) {
      int n = 0;
#pragma unroll
      for (int sgm = 0; sgm < 8; ++sgm) {
#pragma unroll
        for (int dl = 0; dl + sgm < 8; ++dl) {
          if (r == 0 && sgm == 0) MF0(acc[dl], A[0][0], B[0][dl]);
          else MF(acc[sgm + dl], A[r & 1][sgm], B[r & 1][dl]);
          if (r < 3 && (n & 1) == 0 && (n >> 1) < 16) {
            const int s = n >> 2;
            if ((n & 2) == 0) {
              B[(r + 1) & 1][s] = *(const LAS v4u*)(pb + 64 * (r + 1) + 512 * (7 - s));
            } else {
              const v2u a0 = *(const LAS v2u*)(pa + 64 * (r + 1) + 512 * s), a1 = *(const LAS v2u*)(pa + 64 * (r + 1) + 512 * s + 8);
              A[(r + 1) & 1][s] = v4u{a0.x, a0.y, a1.x, a1.y};
            }
          }
          SB();
          ++n;
        }
      }
    }
    MFMA_END();
    STAMP(8);
    BAR();
    LAS unsigned char* rw = lds + L_SCR + w * 8192 + (g4 & 1) * 4096 + (2 * l15 + (g4 >> 1)) * 16;
#pragma unroll
    for (int c = 0; c < 8; ++c) *(LAS f32x4*)(rw + c * 512) = acc[c];
    BAR();
  }
  {
    for (unsigned s = 0; s < SPIN_CAP; ++s) {
      bool ok = true;
#pragma unroll
      for (int i = 0; i < 4; ++i) ok &= sg[i].z == (unsigned)tag && sg[i].w == (unsigned)(tag >> 32);
      if (__all(ok)) break;
      CFENCE();
#pragma unroll
      for (int i = 0; i < 4; ++i) sg[i] = LD128(O_ST1 + (i * 256 + tid) * 16);
    }
    float mu[8], ri[8];
#pragma unroll
    for (int i = 0; i < 4; ++i) {
      mu[2 * i] = lo16(sg[i].x); ri[2 * i] = __builtin_amdgcn_rcpf(hi16(sg[i].x));
      mu[2 * i + 1] = lo16(sg[i].y); ri[2 * i + 1] = __builtin_amdgcn_rcpf(hi16(sg[i].y));
    }
    float yv[2][8], xv[2][8];
#pragma unroll
    for (int c = 0; c < 2; ++c) {
      const v4u kr = *(const LAS v4u*)(lds + L_UR + c * UR_STR + (255 - tid) * 16);
      const float Dc = c ? Dc1 : Dc0;
#pragma unroll
      for (int hf = 0; hf < 2; ++hf) {
        const f32x4 y4 = *(const LAS f32x4*)(lds + L_SCR + (2 * c) * 8192 + hf * 4096 + tid * 16) +
                         *(const LAS f32x4*)(lds + L_SCR + (2 * c + 1) * 8192 + hf * 4096 + tid * 16);
#pragma unroll
        for (int i = 0; i < 4; ++i) {
          const int u = 4 * hf + i;
          const unsigned kd = kr[(7 - u) >> 1], qd = qg[c][u >> 1];
          const float kvv = (u & 1) ? lo16(kd) : hi16(kd);
          const float qv = (u & 1) ? hi16(qd) : lo16(qd);
          const float y = rbf(__builtin_fmaf(kvv, Dc, y4[i]));
          yv[c][u] = y;
          xv[c][u] = __builtin_fmaf((qv - mu[u]) * ri[u], y, qv);
        }
      }
    }
#pragma unroll
    for (int hf = 0; hf < 2; ++hf) {
      const int o = (hf * 32 + (tid >> 3)) * 32768 + p * 128 + (tid & 7) * 16;
      ST128((v4u{pk(xv[0][4 * hf], xv[1][4 * hf]), pk(xv[0][4 * hf + 1], xv[1][4 * hf + 1]), pk(xv[0][4 * hf + 2], xv[1][4 * hf + 2]),
                 pk(xv[0][4 * hf + 3], xv[1][4 * hf + 3])}), O_X1 + o);
      ST128((v4u{pk(yv[0][4 * hf], yv[1][4 * hf]), pk(yv[0][4 * hf + 1], yv[1][4 * hf + 1]), pk(yv[0][4 * hf + 2], yv[1][4 * hf + 2]),
                 pk(yv[0][4 * hf + 3], yv[1][4 * hf + 3])}), O_Y + o);
    }
    STAMP(9);
    DRAIN();
    BAR();
    STAMP(10);
  }
  const v4u wlast = *(const LAS v4u*)(lds + L_BC + lane * 16);
  const unsigned bcv = LD32((tid < 128 ? O_B1 : O_CS) + (112 * jc + ((tid & 127) < 112 ? (tid & 127) : 0)) * 4);
  if (tid < 32) __hip_atomic_fetch_add((gu64*)(ws + O_C1 + (tid * 8 + g) * 128), 1ull, RLX_AGENT);
  v4u w2r[14][4];
  wait_flags((gu64*)(ws + O_FB + (112 + 2 * (14 * w + (lane < 14 ? lane : 0)) + (cs >> 2)) * 64), lane < 14, tag);
#pragma unroll
  for (int i = 0; i < 14; ++i)
#pragma unroll
    for (int nb = 0; nb < 4; ++nb) w2r[i][nb] = LD128S(vl16, O_W2T + (cs * 56 + 14 * w) * 4096, (i * 4 + nb) * 1024);
  SB();
  asm volatile("" :: "a"(wlast));
  BAR();
  *(LAS unsigned*)(lds + L_BC + tid * 4) = bcv;
  if (w == 0) wait_flags((gu64*)(ws + O_C1 + ((b & 31) * 8 + (lane & 7)) * 128), lane < 8, (tag << 8) + 32);
  BAR();
  if (b == 0 && tid == 0) __hip_atomic_store((gu64*)(ws + O_E), tag, RLX_AGENT);
  STAMP(11);

  const int vb = 8 * g + 4 * rh + w;
  const int vbo = 8 * g + 2 * rq + (w >> 1), eo = w & 1;
  unsigned xres[8], yres[8];
  const LAS unsigned char* wl0 = lds + vl16;
  const LAS unsigned char* wl1 = lds + vl16 + 57344;
  asm volatile("" : "+v"(wl0), "+v"(wl1));
#define W1F(f) (*(const LAS v4u*)((f) < 56 ? wl0 + (f) * 1024 : wl1 + ((f) - 56) * 1024))
  v2u b2r[4];
#pragma unroll
  for (int nb = 0; nb < 4; ++nb) b2r[nb] = b2[16 * cs + 4 * nb + g4];

#pragma unroll
  for (int L = 0; L < 2; ++L) {
    const int OX = (L ? O_X3 : O_X1) + vb * 32768;
    const int OH = L ? O_H1 : O_H0;
    v4u F[16][2];
    f32x4 a1[4][2], aS[2], aQ[2];
    float rr[2], mr[2];
    {
      constexpr int WIN = 6;
      v4u raw[16][2];
#pragma unroll
      for (int ks = 0; ks < WIN; ++ks) { raw[ks][0] = LD128S(vl16, OX, ks * 2048); raw[ks][1] = LD128S(vl16, OX, ks * 2048 + 1024); }
      v4u ones = {0x3F803F80u, 0x3F803F80u, 0x3F803F80u, 0x3F803F80u};
      asm volatile("" : "+v"(ones));
      const bool hi = lane & 8;
      v4u W[16][4];
#pragma unroll
      for (int nb = 0; nb < 4; ++nb) W[0][nb] = W1F(nb);
#define SHUF(ks, e, i) do { if (L == 0) { const unsigned a_ = raw[ks][i][e], c_ = raw[ks][i][2 + (e)]; \
        const unsigned ar_ = __builtin_amdgcn_update_dpp(0u, a_, 0x128, 0xf, 0xf, true), cr_ = __builtin_amdgcn_update_dpp(0u, c_, 0x128, 0xf, 0xf, true); \
        F[ks][e][2 * (i)] = hi ? cr_ : a_; F[ks][e][2 * (i) + 1] = hi ? c_ : ar_; } \
        else if ((i) == 0) F[ks][e] = raw[ks][e]; } while (0)
      SHUF(0, 0, 0); SHUF(0, 0, 1); SHUF(0, 1, 0); SHUF(0, 1, 1);
      MFMA_BEGIN();
#pragma unroll
      for (int ks = 0; ks < 16; ++ks) {
#pragma unroll
        for (int n = 0; n < 12; ++n) {
          const int e = n & 1, nb = n >> 1;
          if (n < 8) { if (ks == 0) MF0(a1[nb][e], W[ks][nb], F[ks][e]); else MF(a1[nb][e], W[ks][nb], F[ks][e]); }
          else if (n < 10) { if (ks == 0) MF0(aS[e], ones, F[ks][e]); else MF(aS[e], ones, F[ks][e]); }
          else { if (ks == 0) MF0(aQ[e], F[ks][e], F[ks][e]); else MF(aQ[e], F[ks][e], F[ks][e]); }
          if (ks < 15 && n < 4) W[ks + 1][n] = W1F((ks + 1) * 7 + n);
          if (ks + WIN < 16 && n < 2) raw[ks + WIN][n] = LD128S(vl16, OX, (ks + WIN) * 2048 + n * 1024);
          if (ks < 15 && n >= 3 && n < 11 && (n & 1)) SHUF(ks + 1, ((n - 3) >> 1) & 1, (n - 3) >> 2);
          SB();
        }
      }
      MFMA_END();
    }
    STAMP(12 + 10 * L);
#pragma unroll
    for (int e = 0; e < 2; ++e) {
      const float d = (lane & 2) ? ((lane & 1) ? aQ[e][3] : aQ[e][2]) : ((lane & 1) ? aQ[e][1] : aQ[e][0]);
      const float sq = __uint_as_float(__builtin_amdgcn_ds_bpermute((l15 + 16 * (l15 >> 2)) * 4, __float_as_uint(d)));
      const float mu = aS[e][0] * (1.0f / 512.0f);
      const float var = __builtin_fmaxf(__builtin_fmaf(-mu, mu, sq * (1.0f / 512.0f)), 0.f);
      const float r1 = __builtin_amdgcn_rsqf(var + EPS);
      const float r = r1 * __builtin_amdgcn_rsqf(__builtin_fmaf(var * r1, r1, EPS));
      rr[e] = r;
      mr[e] = -mu * r;
    }
    const int hks = 14 * (jc >> 2), jq = jc & 3;
#define BV(nb) (*(const LAS f32x4*)(lds + L_BC + (16 * (nb) + 4 * g4) * 4))
#define CV(nb) (*(const LAS f32x4*)(lds + L_BC + 512 + (16 * (nb) + 4 * g4) * 4))
#define GELU_PAIR(acc, nb, e, i) pk(gelu(__builtin_fmaf(acc[i], rr[e], __builtin_fmaf(mr[e], CV(nb)[i], BV(nb)[i]))), \
                                   gelu(__builtin_fmaf(acc[(i) + 1], rr[e], __builtin_fmaf(mr[e], CV(nb)[(i) + 1], BV(nb)[(i) + 1]))))
#pragma unroll
    for (int e = 0; e < 2; ++e)
#pragma unroll
      for (int i = 0; i < 2; ++i) {
        const v4u o = {GELU_PAIR(a1[2 * i][e], 2 * i, e, 0), GELU_PAIR(a1[2 * i][e], 2 * i, e, 2),
                       GELU_PAIR(a1[2 * i + 1][e], 2 * i + 1, e, 0), GELU_PAIR(a1[2 * i + 1][e], 2 * i + 1, e, 2)};
        ST128S(o, vl16, OH + (vb * 112 + hks + 2 * jq) * 1024, (e * 56 + i) * 1024);
      }
    STAMP(13 + 10 * L);
    f32x4 a2[3][2];
    {
      v4u W[16][3];
#pragma unroll
      for (int ks = 0; ks < 2; ++ks)
#pragma unroll
        for (int nb = 0; nb < 3; ++nb) W[ks][nb] = W1F(ks * 7 + 4 + nb);
      MFMA_BEGIN();
#pragma unroll
      for (int ks = 0; ks < 16; ++ks) {
#pragma unroll
        for (int n = 0; n < 6; ++n) {
          const int e = n & 1, nb = n >> 1;
          if (ks == 0) MF0(a2[nb][e], W[ks][nb], F[ks][e]);
          else if (ks == 15 && nb == 2) MFA(a2[nb][e], wlast, F[ks][e]);
          else MF(a2[nb][e], W[ks][nb], F[ks][e]);
          if (ks < 14 && e == 0 && !(ks == 13 && nb == 2)) W[ks + 2][nb] = W1F((ks + 2) * 7 + 4 + nb);
          if (ks == 11 && n == 5) {
            DRAIN();
            if (lane == 0) __hip_atomic_store((gu64*)(ws + O_FH + (((L * 2) * 64 + vb) * 16 + jc) * 64), tag, RLX_AGENT);
          }
          SB();
        }
      }
      MFMA_END();
    }
    if (L == 0) {
      const int so = vbo * 32768 + cs * 4096 + eo * 4, vo = g4 * 256 + (l15 & 7) * 16 + (l15 >> 3) * 8;
#pragma unroll
      for (int i = 0; i < 8; ++i) {
        xres[i] = LD32S(vo, O_X1 + so, (i >> 1) * 1024 + (i & 1) * 128);
        yres[i] = LD32S(vo, O_Y + so, (i >> 1) * 1024 + (i & 1) * 128);
      }
    }
    STAMP(14 + 10 * L);
#pragma unroll
    for (int e = 0; e < 2; ++e) {
      const v4u o = {GELU_PAIR(a2[0][e], 4, e, 0), GELU_PAIR(a2[0][e], 4, e, 2), GELU_PAIR(a2[1][e], 5, e, 0), GELU_PAIR(a2[1][e], 5, e, 2)};
      ST128S(o, vl16, OH + (vb * 112 + hks + 8 + jq) * 1024, e * 56 * 1024);
      const v2u o2 = {GELU_PAIR(a2[2][e], 6, e, 0), GELU_PAIR(a2[2][e], 6, e, 2)};
      ST64S(o2, vl8, OH + (vb * 112 + hks + 12 + (jq >> 1)) * 1024 + (jq & 1) * 512, e * 56 * 1024);
    }
    STAMP(15 + 10 * L);
    gu64* fh = (gu64*)(ws + O_FH + (((L * 2) * 64 + 8 * g + 2 * rq + ((lane >> 2) & 1)) * 16 + 4 * w + (lane & 3)) * 64);
    wait_flags(fh, lane < 8, tag);
    DRAIN();
    if (lane == 0) __hip_atomic_store((gu64*)(ws + O_FH + (((L * 2 + 1) * 64 + vb) * 16 + jc) * 64), tag, RLX_AGENT);
    STAMP(16 + 10 * L);
    f32x4 acc[4][4];
    {
      int hb[4];
#pragma unroll
      for (int u = 0; u < 4; ++u) hb[u] = OH + ((16 * g + 4 * rq + ((w + u) & 3)) * 56 + 14 * w) * 1024;
      constexpr int WIN = 3;
      v4u hf[14][4];
#define LOAD_H(ks, u) do { if ((ks) < 12) hf[ks][u] = LD128S(vl16, hb[u], (ks) * 1024); \
        else { const v2u x0 = LD64S(vl8, hb[u], (ks) * 1024), x1 = LD64S(vl8, hb[u], (ks) * 1024 + 512); hf[ks][u] = v4u{x0.x, x0.y, x1.x, x1.y}; } } while (0)
#pragma unroll
      for (int ks = 0; ks < WIN; ++ks)
#pragma unroll
        for (int u = 0; u < 4; ++u) LOAD_H(ks, u);
      MFMA_BEGIN();
#pragma unroll
      for (int ks = 0; ks < 14; ++ks) {
        if (ks + WIN == 8) { wait_flags(fh + 64 * 16 * 8, lane < 8, tag); SB(); }
#pragma unroll
        for (int n = 0; n < 16; ++n) {
          const int u = n >> 2, nb = n & 3;
          if (ks == 0) MFA0(acc[u][nb], w2r[ks][nb], hf[ks][u]); else MFA(acc[u][nb], w2r[ks][nb], hf[ks][u]);
          if (ks + WIN < 14 && nb == 0) LOAD_H(ks + WIN, u);
          SB();
        }
      }
      MFMA_END();
    }
    STAMP(17 + 10 * L);
#pragma unroll
    for (int u = 1; u < 4; ++u)
#pragma unroll
      for (int nb = 0; nb < 4; ++nb)
        *(LAS f32x4*)(lds + L_SCR + (((w + u) & 3) * 3 + (u - 1)) * 4096 + nb * 1024 + lane * 16) = acc[u][nb];
    BAR();
    float xo[16];
#pragma unroll
    for (int nb = 0; nb < 4; ++nb) {
      f32x4 s = acc[0][nb];
#pragma unroll
      for (int u = 0; u < 3; ++u) s += *(const LAS f32x4*)(lds + L_SCR + (w * 3 + u) * 4096 + nb * 1024 + lane * 16);
      xo[4 * nb + 0] = s[0] + lo16(b2r[nb].x) + lo16(xres[2 * nb]);
      xo[4 * nb + 1] = s[1] + hi16(b2r[nb].x) + hi16(xres[2 * nb]);
      xo[4 * nb + 2] = s[2] + lo16(b2r[nb].y) + lo16(xres[2 * nb + 1]);
      xo[4 * nb + 3] = s[3] + hi16(b2r[nb].y) + hi16(xres[2 * nb + 1]);
    }
    STAMP(18 + 10 * L);
    if (L == 0) {
      float s = 0.f;
#pragma unroll
      for (int i = 0; i < 8; ++i) {
        const unsigned t = pk(xo[2 * i], xo[2 * i + 1]);
        xo[2 * i] = lo16(t);
        xo[2 * i + 1] = hi16(t);
        s += xo[2 * i] + xo[2 * i + 1];
      }
      const float mb = sum32x(sum16x(s)) * (1.0f / 64.0f);
      float m2 = 0.f;
#pragma unroll
      for (int i = 0; i < 16; ++i) m2 = __builtin_fmaf(xo[i] - mb, xo[i] - mb, m2);
      m2 = sum32x(sum16x(m2));
      const int so = O_ST2 + (vbo * 2 + eo) * 2048;
      if (lane < 16) ST128S((v4u{__float_as_uint(mb), __float_as_uint(m2), (unsigned)tag, (unsigned)(tag >> 32)}), vl16, so + cs * 256, 0);
      v4u s0, s1;
      for (unsigned it = 0; it < SPIN_CAP; ++it) {
        CFENCE();
        s0 = LD128S(vl16, so, 0);
        s1 = LD128S(vl16, so, 1024);
        if (__all(s0.z == (unsigned)tag && s0.w == (unsigned)(tag >> 32) && s1.z == (unsigned)tag && s1.w == (unsigned)(tag >> 32))) break;
      }
      STAMP(19);
      const float ma = __uint_as_float(s0.x), mc = __uint_as_float(s1.x);
      const float mu = sum32x(sum16x(ma + mc)) * 0.125f;
      const float da = ma - mu, dc = mc - mu;
      const float tot = sum32x(sum16x(__uint_as_float(s0.y) + __uint_as_float(s1.y) + 64.0f * (da * da + dc * dc)));
      const float r = __builtin_amdgcn_rsqf(tot * (1.0f / 512.0f) + EPS);
#pragma unroll
      for (int i = 0; i < 8; ++i)
        xres[i] = pk(__builtin_fmaf((xo[2 * i] - mu) * r, lo16(yres[i]), xo[2 * i]),
                     __builtin_fmaf((xo[2 * i + 1] - mu) * r, hi16(yres[i]), xo[2 * i + 1]));
#pragma unroll
      for (int i = 0; i < 2; ++i)
        ST128S((v4u{xres[4 * i], xres[4 * i + 1], xres[4 * i + 2], xres[4 * i + 3]}), vl16, O_X3 + vbo * 32768 + (4 * cs + eo) * 1024, i * 2048);
      DRAIN();
      if (lane == 0) __hip_atomic_store((gu64*)(ws + O_FX + ((vbo * 2 + eo) * 8 + cs) * 64), tag, RLX_AGENT);
      STAMP(20);
      wait_flags((gu64*)(ws + O_FX + (vb * 16 + l15) * 64), lane < 16, tag);
      STAMP(21);
    } else {
      const int t = 64 * (vbo & 31) + 8 * (l15 & 7) + 4 * (vbo >> 5) + 2 * (l15 >> 3) + eo;
#pragma unroll
      for (int nb = 0; nb < 4; ++nb)
        out[t * 128 + 16 * cs + 4 * nb + g4] = v2u{pk(xo[4 * nb], xo[4 * nb + 1]), pk(xo[4 * nb + 2], xo[4 * nb + 3])};
      STAMP(29);
    }
  }
}

extern "C" void kernel_launch(void* const* d_in, const int* in_sizes, int n_in, void* d_out, int out_size, void* d_ws,
                              size_t ws_size, hipStream_t stream) {
  (void)in_sizes; (void)n_in; (void)out_size; (void)ws_size;
  (void)hipFuncSetAttribute((const void*)h3_mixer_kernel, hipFuncAttributeMaxDynamicSharedMemorySize, L_BYTES);
  h3_mixer_kernel<<<256, 256, L_BYTES, stream>>>(
      (const unsigned*)d_in[0], (const unsigned*)d_in[1], (const unsigned*)d_in[2], (const v4u*)d_in[3], (const v4u*)d_in[4],
      (const u16*)d_in[5], (const unsigned char*)d_in[7], (const u16*)d_in[8], (const unsigned char*)d_in[9], (const v2u*)d_in[10],
      (v2u*)d_out, (unsigned char*)d_ws);
}
```

```cpp
#include <hip/hip_runtime.h>
#include <stdint.h>

#define GAS __attribute__((address_space(1)))
#define LAS __attribute__((address_space(3)))
typedef unsigned v4u __attribute__((ext_vector_type(4)));
typedef unsigned v2u __attribute__((ext_vector_type(2)));
typedef float f32x4 __attribute__((ext_vector_type(4)));
typedef float f32x2 __attribute__((ext_vector_type(2)));
typedef short s16x8 __attribute__((ext_vector_type(8)));
typedef short s16x4 __attribute__((ext_vector_type(4)));
typedef __bf16 bf16x2 __attribute__((ext_vector_type(2)));
typedef unsigned long long u64;
typedef GAS u64 gu64;
typedef unsigned short u16;

#define DBG_STAMP 0

namespace {
constexpr int O_E = 0;
constexpr int O_FA = 4096;
constexpr int O_FB = O_FA + 16384;
constexpr int O_C1 = O_FB + 16384;
constexpr int O_FX = O_C1 + 32768;
constexpr int O_ST1 = O_FX + 65536;
constexpr int O_B1 = O_ST1 + 16384;
constexpr int O_CS = O_B1 + 8192;
constexpr int O_FH = O_CS + 8192;
constexpr int O_ST2 = O_FH + 262144;
constexpr int ROWB = 4224;
constexpr int O_KVR = 1 << 20;
constexpr int O_QT = O_KVR + 512 * ROWB;
constexpr int O_W1T = O_QT + 512 * ROWB;
constexpr int O_W2T = O_W1T + 1835008;
constexpr int O_X1 = O_W2T + 1835008;
constexpr int O_Y = O_X1 + 2097152;
constexpr int O_X3 = O_Y + 2097152;
constexpr int O_H0 = O_X3 + 2097152;
constexpr int O_H1 = O_H0 + 7340032;
constexpr int O_END = O_H1 + 7340032;
constexpr int O_STAMP = O_END;
constexpr int L_W1 = 0, L_SCR = 114688, L_BYTES = 163840;
constexpr int L_BC = 111 * 1024;
constexpr int L_STG_T = 0, L_STG_W = 20480, L_CSP = 40960;
constexpr int HS_STR = 4128, L_HS = L_SCR, L_UR = L_SCR + 8 * HS_STR, UR_STR = 4576;
constexpr unsigned SPIN_CAP = 1u << 16;
constexpr float EPS = 1e-5f;

#define RLX_AGENT __ATOMIC_RELAXED, __HIP_MEMORY_SCOPE_AGENT
#define DRAIN() asm volatile("s_waitcnt vmcnt(0)" ::: "memory")
#define CFENCE() asm volatile("" ::: "memory")
#define SB() __builtin_amdgcn_sched_barrier(0)
#define BAR() do { asm volatile("s_waitcnt lgkmcnt(0)" ::: "memory"); __builtin_amdgcn_s_barrier(); CFENCE(); } while (0)
#define LD128(off) __builtin_amdgcn_raw_buffer_load_b128(rs, (off), 0, 16)
#define LD64(off) __builtin_amdgcn_raw_buffer_load_b64(rs, (off), 0, 16)
#define LD32(off) __builtin_amdgcn_raw_buffer_load_b32(rs, (off), 0, 16)
#define ST128(v, off) ST128S(v, off, 0, 0)
#define ST64(v, off) __builtin_amdgcn_raw_buffer_store_b64((v), rs, (off), 0, 16)
#define ST32(v, off) __builtin_amdgcn_raw_buffer_store_b32((v), rs, (off), 0, 16)
#define LD128S(vo, so, c) __builtin_amdgcn_raw_buffer_load_b128(rs, (vo) + ((c) & 4095), (so) + ((c) & ~4095), 16)
#define LD64S(vo, so, c) __builtin_amdgcn_raw_buffer_load_b64(rs, (vo) + ((c) & 4095), (so) + ((c) & ~4095), 16)
#define LD32S(vo, so, c) __builtin_amdgcn_raw_buffer_load_b32(rs, (vo) + ((c) & 4095), (so) + ((c) & ~4095), 16)
#define ST128S(v, vo, so, c) do { const v4u d_ = (v); __builtin_amdgcn_raw_buffer_store_b128(d_, rs, (vo) + ((c) & 4095), (so) + ((c) & ~4095), 16); \
                                  asm volatile("s_nop 1" :: "v"(d_)); } while (0)
#define ST64S(v, vo, so, c) __builtin_amdgcn_raw_buffer_store_b64((v), rs, (vo) + ((c) & 4095), (so) + ((c) & ~4095), 16)

__device__ __forceinline__ float lo16(unsigned x) { return __uint_as_float(x << 16); }
__device__ __forceinline__ float hi16(unsigned x) { return __uint_as_float(x & 0xffff0000u); }
__device__ __forceinline__ unsigned pk(float a, float b) {
  f32x2 v = {a, b};
  return __builtin_bit_cast(unsigned, __builtin_convertvector(v, bf16x2));
}
__device__ __forceinline__ float rbf(float a) { return lo16(pk(a, a)); }
__device__ __forceinline__ f32x4 mfma(v4u a, v4u b, f32x4 c) {
  return __builtin_amdgcn_mfma_f32_16x16x32_bf16(__builtin_bit_cast(s16x8, a), __builtin_bit_cast(s16x8, b), c, 0, 0, 0);
}
#define MF(acc, a, b) asm volatile("v_mfma_f32_16x16x32_bf16 %0, %1, %2, %0" : "+v"(acc) : "v"(a), "v"(b))
#define MF0(acc, a, b) asm volatile("v_mfma_f32_16x16x32_bf16 %0, %1, %2, 0" : "=&v"(acc) : "v"(a), "v"(b))
#define MFA(acc, a, b) asm volatile("v_mfma_f32_16x16x32_bf16 %0, %1, %2, %0" : "+v"(acc) : "a"(a), "v"(b))
#define MFA0(acc, a, b) asm volatile("v_mfma_f32_16x16x32_bf16 %0, %1, %2, 0" : "=&v"(acc) : "a"(a), "v"(b))
#define MFMA_BEGIN() do { SB(); asm volatile("s_nop 1"); SB(); } while (0)
#define MFMA_END() do { SB(); asm volatile("s_nop 7"); SB(); } while (0)
template <int C> __device__ __forceinline__ float dppf(float v) {
  return __uint_as_float(__builtin_amdgcn_update_dpp(0u, __float_as_uint(v), C, 0xf, 0xf, true));
}
__device__ __forceinline__ float sum16x(float v) {
  auto r = __builtin_amdgcn_permlane16_swap(__float_as_uint(v), __float_as_uint(v), false, false);
  return __uint_as_float(r[0]) + __uint_as_float(r[1]);
}
__device__ __forceinline__ float sum32x(float v) {
  auto r = __builtin_amdgcn_permlane32_swap(__float_as_uint(v), __float_as_uint(v), false, false);
  return __uint_as_float(r[0]) + __uint_as_float(r[1]);
}
__device__ __forceinline__ float wave_sum(float v) {
  v += dppf<0xB1>(v);
  v += dppf<0x4E>(v);
  v += dppf<0x141>(v);
  v += dppf<0x140>(v);
  return sum32x(sum16x(v));
}
__device__ __forceinline__ void wait_flags(gu64* p, bool act, u64 want) {
  for (unsigned s = 0; s < SPIN_CAP; ++s) {
    const u64 x = act ? __hip_atomic_load(p, RLX_AGENT) : want;
    if (__all(x == want)) break;
  }
  CFENCE();
}
__device__ __forceinline__ float gelu(float x) {
  const float z = x * __builtin_fmaf(x * x, -0.10294324f, -2.3022082f);
  return x * __builtin_amdgcn_rcpf(1.0f + __builtin_amdgcn_exp2f(z));
}

#define STAMP(i) do { } while (0)

struct Ctx {
  __amdgpu_buffer_rsrc_t rs;
  GAS unsigned char* ws;
  LAS unsigned char* lds;
  u64 tag;
  int lane, w, g, rh, jc, rq, cs;
};

}

__global__ void __launch_bounds__(256, 1)
h3_mixer_kernel(const unsigned* __restrict__ q, const unsigned* __restrict__ k, const unsigned* __restrict__ v,
                const v4u* __restrict__ sk, const v4u* __restrict__ hk, const u16* __restrict__ Dv,
                const unsigned char* __restrict__ w1, const u16* __restrict__ b1, const unsigned char* __restrict__ w2,
                const v2u* __restrict__ b2, v2u* __restrict__ out, unsigned char* wsp) {
  extern __shared__ __attribute__((aligned(16))) unsigned char lds_raw[];
  LAS unsigned char* lds = (LAS unsigned char*)lds_raw;
  GAS unsigned char* ws = (GAS unsigned char*)wsp;
  const __amdgpu_buffer_rsrc_t rs = __builtin_amdgcn_make_buffer_rsrc(wsp, 0, O_END, 0x00020000);
  const int b = blockIdx.x, tid = threadIdx.x, lane = tid & 63, w = __builtin_amdgcn_readfirstlane(tid >> 6);
  const int g = b & 7, j = b >> 3;
  const int l15 = lane & 15, g4 = lane >> 4;
  const int rh = j >> 4, jc = j & 15, rq = j >> 3, cs = j & 7;
  const int p = g * 32 + j;
  const int vl16 = lane * 16, vl8 = lane * 8;
  STAMP(0);

  const int cp = tid & 31, tq = tid >> 5;
  unsigned kk[11], vv[8], qq[8];
  {
    const int r0 = 64 * j + 8 * tq, col = 32 * g + cp;
#pragma unroll
    for (int i = 0; i < 11; ++i) {
      const int r = r0 - 3 + i;
      kk[i] = k[(r < 0 ? 0 : r) * 256 + col];
    }
#pragma unroll
    for (int i = 0; i < 8; ++i) vv[i] = v[(r0 + i) * 256 + col];
#pragma unroll
    for (int i = 0; i < 8; ++i) qq[i] = q[(r0 + i) * 256 + col];
    if (r0 == 0) kk[0] = kk[1] = kk[2] = 0;
  }
  const v4u tap = sk[32 * g + cp];
  const u64 e0 = __hip_atomic_load((gu64*)(ws + O_E), RLX_AGENT);
  unsigned touch = 0;
  SB();
  v4u qrow[2], hg[2][2], wraw[4];
#pragma unroll
  for (int i = 0; i < 2; ++i) qrow[i] = ((const v4u*)q)[(8 * b + 2 * w + i) * 64 + lane];
#pragma unroll
  for (int c = 0; c < 2; ++c) {
    hg[c][0] = hk[(2 * p + c) * 256 + (tid ? tid - 1 : 0)];
    hg[c][1] = hk[(2 * p + c) * 256 + tid];
  }
  int ksg = 0;
  if (b < 112) {
#pragma unroll
    for (int i = 0; i < 4; ++i)
      wraw[i] = *(const v4u*)(w1 + (32 * (4 * w + i) + (lane >> 1)) * 3584 + b * 32 + (lane & 1) * 16);
  } else {
    const int u = b < 224 ? b - 112 : 0;
    ksg = u >> 1;
    const int wq = ksg / 14, kl = ksg - 14 * wq;
    const int rowA = kl < 8 ? 112 * (4 * wq + (kl >> 1)) + 32 * (kl & 1)
                            : (kl < 12 ? 112 * (4 * wq + kl - 8) + 64 : 112 * (4 * wq + 2 * (kl - 12)) + 96);
    const int dB = kl < 12 ? 0 : 96;
#pragma unroll
    for (int i = 0; i < 4; ++i)
      wraw[i] = *(const v4u*)(w2 + (rowA + 8 * i + (lane >> 3) + (i >= 2 ? dB : 0)) * 1024 + (u & 1) * 512 + w * 128 + (lane & 7) * 16);
  }
  const float Dc0 = lo16(Dv[2 * p]), Dc1 = lo16(Dv[2 * p + 1]);
  SB();
  const u64 tag = ((u64)__builtin_amdgcn_readfirstlane((unsigned)(e0 >> 32)) << 32 | __builtin_amdgcn_readfirstlane((unsigned)e0)) +
                  0x9E3779B97F4A7C15ull;
  {
    const f32x2 t0 = {lo16(tap.x), lo16(tap.z)}, t1 = {hi16(tap.x), hi16(tap.z)}, t2 = {lo16(tap.y), lo16(tap.w)},
                t3 = {hi16(tap.y), hi16(tap.w)};
    f32x2 kf[11], r[8];
#pragma unroll
    for (int i = 0; i < 11; ++i) kf[i] = f32x2{lo16(kk[i]), hi16(kk[i])};
#pragma unroll
    for (int u = 0; u < 8; ++u)
      r[u] = (kf[u] * t3 + kf[u + 1] * t2 + kf[u + 2] * t1 + kf[u + 3] * t0) * f32x2{lo16(vv[u]), hi16(vv[u])};
    STAMP(1);
    LAS unsigned char* st = lds + L_STG_T + (2 * cp) * 144;
    *(LAS v4u*)(st + (7 - tq) * 16) = v4u{pk(r[7].x, r[6].x), pk(r[5].x, r[4].x), pk(r[3].x, r[2].x), pk(r[1].x, r[0].x)};
    *(LAS v4u*)(st + 144 + (7 - tq) * 16) = v4u{pk(r[7].y, r[6].y), pk(r[5].y, r[4].y), pk(r[3].y, r[2].y), pk(r[1].y, r[0].y)};
    *(LAS v4u*)(st + 9216 + tq * 16) =
        v4u{__builtin_amdgcn_perm(qq[1], qq[0], 0x05040100u), __builtin_amdgcn_perm(qq[3], qq[2], 0x05040100u),
            __builtin_amdgcn_perm(qq[5], qq[4], 0x05040100u), __builtin_amdgcn_perm(qq[7], qq[6], 0x05040100u)};
    *(LAS v4u*)(st + 9216 + 144 + tq * 16) =
        v4u{__builtin_amdgcn_perm(qq[1], qq[0], 0x07060302u), __builtin_amdgcn_perm(qq[3], qq[2], 0x07060302u),
            __builtin_amdgcn_perm(qq[5], qq[4], 0x07060302u), __builtin_amdgcn_perm(qq[7], qq[6], 0x07060302u)};
    BAR();
#pragma unroll
    for (int i = 0; i < 2; ++i) {
      const int ch = (tid >> 3) + 32 * i, pos = tid & 7;
      const v4u a = *(LAS v4u*)(lds + L_STG_T + ch * 144 + pos * 16);
      const v4u c = *(LAS v4u*)(lds + L_STG_T + 9216 + ch * 144 + pos * 16);
      ST128(a, O_KVR + (64 * g + ch) * ROWB + (248 - 8 * j + pos) * 16);
      ST128(c, O_QT + (64 * g + ch) * ROWB + (8 * j + pos) * 16);
    }
    if (j == 0 && tid < 32) __hip_atomic_store((gu64*)(ws + O_C1 + (tid * 8 + g) * 128), tag << 8, RLX_AGENT);
    STAMP(2);
    asm volatile("" :: "v"(touch));
    DRAIN();
    BAR();
    if (tid == 0) __hip_atomic_store((gu64*)(ws + O_FA + b * 64), tag, RLX_AGENT);
    STAMP(3);
  }
  {
    float ms[2][2];
#pragma unroll
    for (int i = 0; i < 2; ++i) {
      float x[8];
#pragma unroll
      for (int d = 0; d < 4; ++d) { x[2 * d] = lo16(qrow[i][d]); x[2 * d + 1] = hi16(qrow[i][d]); }
      float s = ((x[0] + x[1]) + (x[2] + x[3])) + ((x[4] + x[5]) + (x[6] + x[7]));
      const float m = wave_sum(s) * (1.0f / 512.0f);
      float sq = 0.f;
#pragma unroll
      for (int d = 0; d < 8; ++d) sq = __builtin_fmaf(x[d] - m, x[d] - m, sq);
      const float var = wave_sum(sq) * (1.0f / 512.0f);
      ms[i][0] = m;
      ms[i][1] = __builtin_sqrtf(rbf(rbf(var) + EPS));
    }
    if (lane == 0) ST128((v4u{pk(ms[0][0], ms[0][1]), pk(ms[1][0], ms[1][1]), (unsigned)tag, (unsigned)(tag >> 32)}), O_ST1 + (w * 256 + b) * 16);
    if (b < 224) {
#pragma unroll
      for (int i = 0; i < 4; ++i) *(LAS v4u*)(lds + L_STG_W + (4 * w + i) * 1024 + lane * 16) = wraw[i];
      const int tq4 = (lane >> 2) & 3, tp = lane & 3;
      if (b < 112) {
        const int jcp = b / 7, nbp = b - 7 * jcp;
        f32x4 cacc;
        v4u ones = {0x3F803F80u, 0x3F803F80u, 0x3F803F80u, 0x3F803F80u};
      asm volatile("" : "+v"(ones));
#pragma unroll
        for (int i = 0; i < 4; ++i) {
          LAS unsigned char* a = lds + L_STG_W + (4 * w + i) * 1024 + (4 * g4 + tq4) * 32 + tp * 8;
          const v2u x0 = __builtin_bit_cast(v2u, __builtin_amdgcn_ds_read_tr16_b64_v4i16((LAS s16x4*)a));
          const v2u x1 = __builtin_bit_cast(v2u, __builtin_amdgcn_ds_read_tr16_b64_v4i16((LAS s16x4*)(a + 512)));
          const v4u f = {x0.x, x0.y, x1.x, x1.y};
          ST128(f, O_W1T + ((jcp * 16 + 4 * w + i) * 7 + nbp) * 1024 + lane * 16);
          MFMA_BEGIN();
          if (i == 0) MF0(cacc, f, ones); else MF(cacc, f, ones);
        }
        MFMA_END();
        if (l15 == 0) *(LAS f32x4*)(lds + L_CSP + w * 64 + g4 * 16) = cacc;
      } else {
        const int csp = 4 * ((b - 112) & 1) + w;
#pragma unroll
        for (int nb = 0; nb < 4; ++nb) {
          LAS unsigned char* a = lds + L_STG_W + w * 4096 + (4 * g4 + tq4) * 128 + nb * 32 + tp * 8;
          const v2u x0 = __builtin_bit_cast(v2u, __builtin_amdgcn_ds_read_tr16_b64_v4i16((LAS s16x4*)a));
          const v2u x1 = __builtin_bit_cast(v2u, __builtin_amdgcn_ds_read_tr16_b64_v4i16((LAS s16x4*)(a + 2048)));
          ST128((v4u{x0.x, x0.y, x1.x, x1.y}), O_W2T + ((csp * 56 + ksg) * 4 + nb) * 1024 + lane * 16);
        }
      }
    }
#pragma unroll
    for (int c = 0; c < 2; ++c) {
      v4u a = hg[c][0];
      const v4u d = hg[c][1];
      if (tid == 0) a = v4u{0, 0, 0, 0};
      const unsigned s0 = __builtin_amdgcn_alignbyte(a.y, a.x, 2), s1 = __builtin_amdgcn_alignbyte(a.z, a.y, 2),
                     s2 = __builtin_amdgcn_alignbyte(a.w, a.z, 2), s3 = __builtin_amdgcn_alignbyte(d.x, a.w, 2),
                     s4 = __builtin_amdgcn_alignbyte(d.y, d.x, 2);
      LAS unsigned char* o = lds + L_HS + c * 4 * HS_STR + (tid + 1) * 16;
      *(LAS v4u*)(o) = v4u{s0, s1, s2, s3};
      *(LAS v4u*)(o + HS_STR) = v4u{a.y, a.z, a.w, d.x};
      *(LAS v4u*)(o + 2 * HS_STR) = v4u{s1, s2, s3, s4};
      *(LAS v4u*)(o + 3 * HS_STR) = v4u{a.z, a.w, d.x, d.y};
      if (tid == 255) {
        const unsigned u0 = __builtin_amdgcn_alignbyte(d.y, d.x, 2), u1 = __builtin_amdgcn_alignbyte(d.z, d.y, 2),
                       u2 = __builtin_amdgcn_alignbyte(d.w, d.z, 2), u3 = d.w >> 16;
        *(LAS v4u*)(o + 16) = v4u{u0, u1, u2, u3};
        *(LAS v4u*)(o + 16 + HS_STR) = v4u{d.y, d.z, d.w, 0};
        *(LAS v4u*)(o + 16 + 2 * HS_STR) = v4u{u1, u2, u3, 0};
        *(LAS v4u*)(o + 16 + 3 * HS_STR) = v4u{d.z, d.w, 0, 0};
      }
    }
    if (tid < 8) *(LAS v4u*)(lds + L_HS + tid * HS_STR) = v4u{0, 0, 0, 0};
    if (tid >= 64 && tid < 124) {
      const int i = tid - 64, c = i >= 30;
      *(LAS v4u*)(lds + L_UR + c * UR_STR + 4096 + (i - 30 * c) * 16) = v4u{0, 0, 0, 0};
    }
    STAMP(4);
    DRAIN();
    BAR();
    STAMP(5);
  }
  if (w == 0) {
    wait_flags((gu64*)(ws + O_FA + ((lane & 31) * 8 + g) * 64), lane < 32, tag);
    STAMP(6);
    v4u u[8];
#pragma unroll
    for (int i = 0; i < 8; ++i) u[i] = LD128S(vl16, O_KVR + (2 * p + (i >> 2)) * ROWB, (i & 3) * 1024);
#pragma unroll
    for (int i = 0; i < 8; ++i) *(LAS v4u*)(lds + L_UR + (i >> 2) * UR_STR + (i & 3) * 1024 + lane * 16) = u[i];
  } else {
    if (w == 1) {
      if (b < 112 && lane < 16) {
        const LAS float* cp4 = (const LAS float*)(lds + L_CSP);
        const float s = (cp4[lane] + cp4[16 + lane]) + (cp4[32 + lane] + cp4[48 + lane]);
        ST32(__float_as_uint(s), O_CS + (16 * b + lane) * 4);
        ST32((unsigned)b1[16 * b + lane] << 16, O_B1 + (16 * b + lane) * 4);
      }
      asm volatile("s_waitcnt vmcnt(0) lgkmcnt(0)" ::: "memory");
      if (lane == 0) __hip_atomic_store((gu64*)(ws + O_FB + b * 64), tag, RLX_AGENT);
    }
    wait_flags((gu64*)(ws + O_FB + (7 * jc + (lane & 7)) * 64), lane < 7, tag);
    STAMP(6);
  }
  BAR();
  STAMP(7);
  if (w != 0) {
    const unsigned voff = lane * 16;
    for (int gi = w - 1; gi < 28; gi += 3) {
      const u64 src = (u64)(uintptr_t)wsp + O_W1T + jc * 114688 + gi * 4096;
      const unsigned dst = (unsigned)(uintptr_t)lds + L_W1 + gi * 4096;
      unsigned keep;
      asm volatile(
          "s_mov_b32 %0, m0\n\ts_mov_b32 m0, %3\n\ts_nop 0\n\t"
          "global_load_lds_dwordx4 %1, %2 sc1\n\t"
          "global_load_lds_dwordx4 %1, %2 offset:1024 sc1\n\t"
          "global_load_lds_dwordx4 %1, %2 offset:2048 sc1\n\t"
          "global_load_lds_dwordx4 %1, %2 offset:3072 sc1\n\t"
          "s_mov_b32 m0, %0"
          : "=&s"(keep) : "v"(voff), "s"(src), "s"(dst) : "memory");
    }
  }
  v4u qg[2], sg[4];
#pragma unroll
  for (int c = 0; c < 2; ++c) qg[c] = LD128(O_QT + (2 * p + c) * ROWB + tid * 16);
#pragma unroll
  for (int i = 0; i < 4; ++i) sg[i] = LD128(O_ST1 + (i * 256 + tid) * 16);
  SB();
  {
    f32x4 acc[8];
    const int ch = w >> 1, rho0 = 4 * (w & 1);
    const LAS unsigned char* pa = lds + L_HS + (ch * 4 + (lane & 3)) * HS_STR + 8 * ((l15 >> 2) + 2 * g4) + 64 * rho0;
    const LAS unsigned char* pb = lds + L_UR + ch * UR_STR + 2 * (240 - 16 * l15 + 8 * g4) + 64 * rho0;
    v4u A[2][8], B[2][8];
#pragma unroll
    for (int s = 0; s < 8; ++s) {
      const v2u a0 = *(const LAS v2u*)(pa + 512 * s), a1 = *(const LAS v2u*)(pa + 512 * s + 8);
      A[0][s] = v4u{a0.x, a0.y, a1.x, a1.y};
      B[0][s] = *(const LAS v4u*)(pb + 512 * (7 - s));
    }
    MFMA_BEGIN();
#pragma unroll
    for (int r = 0; r < 4; ++r) {
      int n = 0;
#pragma unroll
      for (int sgm = 0; sgm < 8; ++sgm) {
#pragma unroll
        for (int dl = 0; dl + sgm < 8; ++dl) {
          if (r == 0 && sgm == 0) MF0(acc[dl], A[0][0], B[0][dl]);
          else MF(acc[sgm + dl], A[r & 1][sgm], B[r & 1][dl]);
          if (r < 3 && (n & 1) == 0 && (n >> 1) < 16) {
            const int s = n >> 2;
            if ((n & 2) == 0) {
              B[(r + 1) & 1][s] = *(const LAS v4u*)(pb + 64 * (r + 1) + 512 * (7 - s));
            } else {
              const v2u a0 = *(const LAS v2u*)(pa + 64 * (r + 1) + 512 * s), a1 = *(const LAS v2u*)(pa + 64 * (r + 1) + 512 * s + 8);
              A[(r + 1) & 1][s] = v4u{a0.x, a0.y, a1.x, a1.y};
            }
          }
          SB();
          ++n;
        }
      }
    }
    MFMA_END();
    STAMP(8);
    BAR();
    LAS unsigned char* rw = lds + L_SCR + w * 8192 + (g4 & 1) * 4096 + (2 * l15 + (g4 >> 1)) * 16;
#pragma unroll
    for (int c = 0; c < 8; ++c) *(LAS f32x4*)(rw + c * 512) = acc[c];
    BAR();
  }
  {
    for (unsigned s = 0; s < SPIN_CAP; ++s) {
      bool ok = true;
#pragma unroll
      for (int i = 0; i < 4; ++i) ok &= sg[i].z == (unsigned)tag && sg[i].w == (unsigned)(tag >> 32);
      if (__all(ok)) break;
      CFENCE();
#pragma unroll
      for (int i = 0; i < 4; ++i) sg[i] = LD128(O_ST1 + (i * 256 + tid) * 16);
    }
    float mu[8], ri[8];
#pragma unroll
    for (int i = 0; i < 4; ++i) {
      mu[2 * i] = lo16(sg[i].x); ri[2 * i] = __builtin_amdgcn_rcpf(hi16(sg[i].x));
      mu[2 * i + 1] = lo16(sg[i].y); ri[2 * i + 1] = __builtin_amdgcn_rcpf(hi16(sg[i].y));
    }
    float yv[2][8], xv[2][8];
#pragma unroll
    for (int c = 0; c < 2; ++c) {
      const v4u kr = *(const LAS v4u*)(lds + L_UR + c * UR_STR + (255 - tid) * 16);
      const float Dc = c ? Dc1 : Dc0;
#pragma unroll
      for (int hf = 0; hf < 2; ++hf) {
        const f32x4 y4 = *(const LAS f32x4*)(lds + L_SCR + (2 * c) * 8192 + hf * 4096 + tid * 16) +
                         *(const LAS f32x4*)(lds + L_SCR + (2 * c + 1) * 8192 + hf * 4096 + tid * 16);
#pragma unroll
        for (int i = 0; i < 4; ++i) {
          const int u = 4 * hf + i;
          const unsigned kd = kr[(7 - u) >> 1], qd = qg[c][u >> 1];
          const float kvv = (u & 1) ? lo16(kd) : hi16(kd);
          const float qv = (u & 1) ? hi16(qd) : lo16(qd);
          const float y = rbf(__builtin_fmaf(kvv, Dc, y4[i]));
          yv[c][u] = y;
          xv[c][u] = __builtin_fmaf((qv - mu[u]) * ri[u], y, qv);
        }
      }
    }
#pragma unroll
    for (int hf = 0; hf < 2; ++hf) {
      const int o = (hf * 32 + (tid >> 3)) * 32768 + p * 128 + (tid & 7) * 16;
      ST128((v4u{pk(xv[0][4 * hf], xv[1][4 * hf]), pk(xv[0][4 * hf + 1], xv[1][4 * hf + 1]), pk(xv[0][4 * hf + 2], xv[1][4 * hf + 2]),
                 pk(xv[0][4 * hf + 3], xv[1][4 * hf + 3])}), O_X1 + o);
      ST128((v4u{pk(yv[0][4 * hf], yv[1][4 * hf]), pk(yv[0][4 * hf + 1], yv[1][4 * hf + 1]), pk(yv[0][4 * hf + 2], yv[1][4 * hf + 2]),
                 pk(yv[0][4 * hf + 3], yv[1][4 * hf + 3])}), O_Y + o);
    }
    STAMP(9);
    DRAIN();
    BAR();
    STAMP(10);
  }
  const v4u wlast = *(const LAS v4u*)(lds + L_BC + lane * 16);
  const unsigned bcv = LD32((tid < 128 ? O_B1 : O_CS) + (112 * jc + ((tid & 127) < 112 ? (tid & 127) : 0)) * 4);
  if (tid < 32) __hip_atomic_fetch_add((gu64*)(ws + O_C1 + (tid * 8 + g) * 128), 1ull, RLX_AGENT);
  v4u w2r[14][4];
  wait_flags((gu64*)(ws + O_FB + (112 + 2 * (14 * w + (lane < 14 ? lane : 0)) + (cs >> 2)) * 64), lane < 14, tag);
#pragma unroll
  for (int i = 0; i < 14; ++i)
#pragma unroll
    for (int nb = 0; nb < 4; ++nb) w2r[i][nb] = LD128S(vl16, O_W2T + (cs * 56 + 14 * w) * 4096, (i * 4 + nb) * 1024);
  SB();
  asm volatile("" :: "a"(wlast));
  BAR();
  *(LAS unsigned*)(lds + L_BC + tid * 4) = bcv;
  if (w == 0) wait_flags((gu64*)(ws + O_C1 + ((b & 31) * 8 + (lane & 7)) * 128), lane < 8, (tag << 8) + 32);
  BAR();
  if (b == 0 && tid == 0) __hip_atomic_store((gu64*)(ws + O_E), tag, RLX_AGENT);
  STAMP(11);

  const int vb = 8 * g + 4 * rh + w;
  const int vbo = 8 * g + 2 * rq + (w >> 1), eo = w & 1;
  unsigned xres[8], yres[8];
  const LAS unsigned char* wl0 = lds + vl16;
  const LAS unsigned char* wl1 = lds + vl16 + 57344;
  asm volatile("" : "+v"(wl0), "+v"(wl1));
#define W1F(f) (*(const LAS v4u*)((f) < 56 ? wl0 + (f) * 1024 : wl1 + ((f) - 56) * 1024))
  v2u b2r[4];
#pragma unroll
  for (int nb = 0; nb < 4; ++nb) b2r[nb] = b2[16 * cs + 4 * nb + g4];

#pragma unroll
  for (int L = 0; L < 2; ++L) {
    const int OX = (L ? O_X3 : O_X1) + vb * 32768;
    const int OH = L ? O_H1 : O_H0;
    v4u F[16][2];
    f32x4 a1[4][2], aS[2], aQ[2];
    float rr[2], mr[2];
    {
      constexpr int WIN = 6;
      v4u raw[16][2];
#pragma unroll
      for (int ks = 0; ks < WIN; ++ks) { raw[ks][0] = LD128S(vl16, OX, ks * 2048); raw[ks][1] = LD128S(vl16, OX, ks * 2048 + 1024); }
      v4u ones = {0x3F803F80u, 0x3F803F80u, 0x3F803F80u, 0x3F803F80u};
      asm volatile("" : "+v"(ones));
      const bool hi = lane & 8;
      v4u W[16][4];
#pragma unroll
      for (int nb = 0; nb < 4; ++nb) W[0][nb] = W1F(nb);
#define SHUF(ks, e, i) do { if (L == 0) { const unsigned a_ = raw[ks][i][e], c_ = raw[ks][i][2 + (e)]; \
        const unsigned ar_ = __builtin_amdgcn_update_dpp(0u, a_, 0x128, 0xf, 0xf, true), cr_ = __builtin_amdgcn_update_dpp(0u, c_, 0x128, 0xf, 0xf, true); \
        F[ks][e][2 * (i)] = hi ? cr_ : a_; F[ks][e][2 * (i) + 1] = hi ? c_ : ar_; } \
        else if ((i) == 0) F[ks][e] = raw[ks][e]; } while (0)
      SHUF(0, 0, 0); SHUF(0, 0, 1); SHUF(0, 1, 0); SHUF(0, 1, 1);
      MFMA_BEGIN();
#pragma unroll
      for (int ks = 0; ks < 16; ++ks) {
#pragma unroll
        for (int n = 0; n < 12; ++n) {
          const int e = n & 1, nb = n >> 1;
          if (n < 8) { if (ks == 0) MF0(a1[nb][e], W[ks][nb], F[ks][e]); else MF(a1[nb][e], W[ks][nb], F[ks][e]); }
          else if (n < 10) { if (ks == 0) MF0(aS[e], ones, F[ks][e]); else MF(aS[e], ones, F[ks][e]); }
          else { if (ks == 0) MF0(aQ[e], F[ks][e], F[ks][e]); else MF(aQ[e], F[ks][e], F[ks][e]); }
          if (ks < 15 && n < 4) W[ks + 1][n] = W1F((ks + 1) * 7 + n);
          if (ks + WIN < 16 && n < 2) raw[ks + WIN][n] = LD128S(vl16, OX, (ks + WIN) * 2048 + n * 1024);
          if (ks < 15 && n >= 3 && n < 11 && (n & 1)) SHUF(ks + 1, ((n - 3) >> 1) & 1, (n - 3) >> 2);
          SB();
        }
      }
      MFMA_END();
    }
    STAMP(12 + 10 * L);
#pragma unroll
    for (int e = 0; e < 2; ++e) {
      const float d = (lane & 2) ? ((lane & 1) ? aQ[e][3] : aQ[e][2]) : ((lane & 1) ? aQ[e][1] : aQ[e][0]);
      const float sq = __uint_as_float(__builtin_amdgcn_ds_bpermute((l15 + 16 * (l15 >> 2)) * 4, __float_as_uint(d)));
      const float mu = aS[e][0] * (1.0f / 512.0f);
      const float var = __builtin_fmaxf(__builtin_fmaf(-mu, mu, sq * (1.0f / 512.0f)), 0.f);
      const float r1 = __builtin_amdgcn_rsqf(var + EPS);
      const float r = r1 * __builtin_amdgcn_rsqf(__builtin_fmaf(var * r1, r1, EPS));
      rr[e] = r;
      mr[e] = -mu * r;
    }
    const int hks = 14 * (jc >> 2), jq = jc & 3;
#define BV(nb) (*(const LAS f32x4*)(lds + L_BC + (16 * (nb) + 4 * g4) * 4))
#define CV(nb) (*(const LAS f32x4*)(lds + L_BC + 512 + (16 * (nb) + 4 * g4) * 4))
#define GELU_PAIR(acc, nb, e, i) pk(gelu(__builtin_fmaf(acc[i], rr[e], __builtin_fmaf(mr[e], CV(nb)[i], BV(nb)[i]))), \
                                   gelu(__builtin_fmaf(acc[(i) + 1], rr[e], __builtin_fmaf(mr[e], CV(nb)[(i) + 1], BV(nb)[(i) + 1]))))
#pragma unroll
    for (int e = 0; e < 2; ++e)
#pragma unroll
      for (int i = 0; i < 2; ++i) {
        const v4u o = {GELU_PAIR(a1[2 * i][e], 2 * i, e, 0), GELU_PAIR(a1[2 * i][e], 2 * i, e, 2),
                       GELU_PAIR(a1[2 * i + 1][e], 2 * i + 1, e, 0), GELU_PAIR(a1[2 * i + 1][e], 2 * i + 1, e, 2)};
        ST128S(o, vl16, OH + (vb * 112 + hks + 2 * jq) * 1024, (e * 56 + i) * 1024);
      }
    STAMP(13 + 10 * L);
    f32x4 a2[3][2];
    {
      v4u W[16][3];
#pragma unroll
      for (int ks = 0; ks < 2; ++ks)
#pragma unroll
        for (int nb = 0; nb < 3; ++nb) W[ks][nb] = W1F(ks * 7 + 4 + nb);
      MFMA_BEGIN();
#pragma unroll
      for (int ks = 0; ks < 16; ++ks) {
#pragma unroll
        for (int n = 0; n < 6; ++n) {
          const int e = n & 1, nb = n >> 1;
          if (ks == 0) MF0(a2[nb][e], W[ks][nb], F[ks][e]);
          else if (ks == 15 && nb == 2) MFA(a2[nb][e], wlast, F[ks][e]);
          else MF(a2[nb][e], W[ks][nb], F[ks][e]);
          if (ks < 14 && e == 0 && !(ks == 13 && nb == 2)) W[ks + 2][nb] = W1F((ks + 2) * 7 + 4 + nb);
          if (ks == 11 && n == 5) {
            DRAIN();
            if (lane == 0) __hip_atomic_store((gu64*)(ws + O_FH + (((L * 2) * 64 + vb) * 16 + jc) * 64), tag, RLX_AGENT);
          }
          SB();
        }
      }
      MFMA_END();
    }
    if (L == 0) {
      const int so = vbo * 32768 + cs * 4096 + eo * 4, vo = g4 * 256 + (l15 & 7) * 16 + (l15 >> 3) * 8;
#pragma unroll
      for (int i = 0; i < 8; ++i) {
        xres[i] = LD32S(vo, O_X1 + so, (i >> 1) * 1024 + (i & 1) * 128);
        yres[i] = LD32S(vo, O_Y + so, (i >> 1) * 1024 + (i & 1) * 128);
      }
    }
    STAMP(14 + 10 * L);
#pragma unroll
    for (int e = 0; e < 2; ++e) {
      const v4u o = {GELU_PAIR(a2[0][e], 4, e, 0), GELU_PAIR(a2[0][e], 4, e, 2), GELU_PAIR(a2[1][e], 5, e, 0), GELU_PAIR(a2[1][e], 5, e, 2)};
      ST128S(o, vl16, OH + (vb * 112 + hks + 8 + jq) * 1024, e * 56 * 1024);
      const v2u o2 = {GELU_PAIR(a2[2][e], 6, e, 0), GELU_PAIR(a2[2][e], 6, e, 2)};
      ST64S(o2, vl8, OH + (vb * 112 + hks + 12 + (jq >> 1)) * 1024 + (jq & 1) * 512, e * 56 * 1024);
    }
    STAMP(15 + 10 * L);
    gu64* fh = (gu64*)(ws + O_FH + (((L * 2) * 64 + 8 * g + 2 * rq + ((lane >> 2) & 1)) * 16 + 4 * w + (lane & 3)) * 64);
    wait_flags(fh, lane < 8, tag);
    DRAIN();
    if (lane == 0) __hip_atomic_store((gu64*)(ws + O_FH + (((L * 2 + 1) * 64 + vb) * 16 + jc) * 64), tag, RLX_AGENT);
    STAMP(16 + 10 * L);
    f32x4 acc[4][4];
    {
      int hb[4];
#pragma unroll
      for (int u = 0; u < 4; ++u) hb[u] = OH + ((16 * g + 4 * rq + ((w + u) & 3)) * 56 + 14 * w) * 1024;
      constexpr int WIN = 3;
      v4u hf[14][4];
#define LOAD_H(ks, u) do { if ((ks) < 12) hf[ks][u] = LD128S(vl16, hb[u], (ks) * 1024); \
        else { const v2u x0 = LD64S(vl8, hb[u], (ks) * 1024), x1 = LD64S(vl8, hb[u], (ks) * 1024 + 512); hf[ks][u] = v4u{x0.x, x0.y, x1.x, x1.y}; } } while (0)
#pragma unroll
      for (int ks = 0; ks < WIN; ++ks)
#pragma unroll
        for (int u = 0; u < 4; ++u) LOAD_H(ks, u);
      MFMA_BEGIN();
#pragma unroll
      for (int ks = 0; ks < 14; ++ks) {
        if (ks + WIN == 8) { wait_flags(fh + 64 * 16 * 8, lane < 8, tag); SB(); }
#pragma unroll
        for (int n = 0; n < 16; ++n) {
          const int u = n >> 2, nb = n & 3;
          if (ks == 0) MFA0(acc[u][nb], w2r[ks][nb], hf[ks][u]); else MFA(acc[u][nb], w2r[ks][nb], hf[ks][u]);
          if (ks + WIN < 14 && nb == 0) LOAD_H(ks + WIN, u);
          SB();
        }
      }
      MFMA_END();
    }
    STAMP(17 + 10 * L);
#pragma unroll
    for (int u = 1; u < 4; ++u)
#pragma unroll
      for (int nb = 0; nb < 4; ++nb)
        *(LAS f32x4*)(lds + L_SCR + (((w + u) & 3) * 3 + (u - 1)) * 4096 + nb * 1024 + lane * 16) = acc[u][nb];
    BAR();
    float xo[16];
#pragma unroll
    for (int nb = 0; nb < 4; ++nb) {
      f32x4 s = acc[0][nb];
#pragma unroll
      for (int u = 0; u < 3; ++u) s += *(const LAS f32x4*)(lds + L_SCR + (w * 3 + u) * 4096 + nb * 1024 + lane * 16);
      xo[4 * nb + 0] = s[0] + lo16(b2r[nb].x) + lo16(xres[2 * nb]);
      xo[4 * nb + 1] = s[1] + hi16(b2r[nb].x) + hi16(xres[2 * nb]);
      xo[4 * nb + 2] = s[2] + lo16(b2r[nb].y) + lo16(xres[2 * nb + 1]);
      xo[4 * nb + 3] = s[3] + hi16(b2r[nb].y) + hi16(xres[2 * nb + 1]);
    }
    STAMP(18 + 10 * L);
    if (L == 0) {
      float s = 0.f;
#pragma unroll
      for (int i = 0; i < 8; ++i) {
        const unsigned t = pk(xo[2 * i], xo[2 * i + 1]);
        xo[2 * i] = lo16(t);
        xo[2 * i + 1] = hi16(t);
        s += xo[2 * i] + xo[2 * i + 1];
      }
      const float mb = sum32x(sum16x(s)) * (1.0f / 64.0f);
      float m2 = 0.f;
#pragma unroll
      for (int i = 0; i < 16; ++i) m2 = __builtin_fmaf(xo[i] - mb, xo[i] - mb, m2);
      m2 = sum32x(sum16x(m2));
      const int so = O_ST2 + (vbo * 2 + eo) * 2048;
      if (lane < 16) ST128S((v4u{__float_as_uint(mb), __float_as_uint(m2), (unsigned)tag, (unsigned)(tag >> 32)}), vl16, so + cs * 256, 0);
      v4u s0, s1;
      for (unsigned it = 0; it < SPIN_CAP; ++it) {
        CFENCE();
        s0 = LD128S(vl16, so, 0);
        s1 = LD128S(vl16, so, 1024);
        if (__all(s0.z == (unsigned)tag && s0.w == (unsigned)(tag >> 32) && s1.z == (unsigned)tag && s1.w == (unsigned)(tag >> 32))) break;
      }
      STAMP(19);
      const float ma = __uint_as_float(s0.x), mc = __uint_as_float(s1.x);
      const float mu = sum32x(sum16x(ma + mc)) * 0.125f;
      const float da = ma - mu, dc = mc - mu;
      const float tot = sum32x(sum16x(__uint_as_float(s0.y) + __uint_as_float(s1.y) + 64.0f * (da * da + dc * dc)));
      const float r = __builtin_amdgcn_rsqf(tot * (1.0f / 512.0f) + EPS);
#pragma unroll
      for (int i = 0; i < 8; ++i)
        xres[i] = pk(__builtin_fmaf((xo[2 * i] - mu) * r, lo16(yres[i]), xo[2 * i]),
                     __builtin_fmaf((xo[2 * i + 1] - mu) * r, hi16(yres[i]), xo[2 * i + 1]));
#pragma unroll
      for (int i = 0; i < 2; ++i)
        ST128S((v4u{xres[4 * i], xres[4 * i + 1], xres[4 * i + 2], xres[4 * i + 3]}), vl16, O_X3 + vbo * 32768 + (4 * cs + eo) * 1024, i * 2048);
      DRAIN();
      if (lane == 0) __hip_atomic_store((gu64*)(ws + O_FX + ((vbo * 2 + eo) * 8 + cs) * 64), tag, RLX_AGENT);
      STAMP(20);
      wait_flags((gu64*)(ws + O_FX + (vb * 16 + l15) * 64), lane < 16, tag);
      STAMP(21);
    } else {
      const int t = 64 * (vbo & 31) + 8 * (l15 & 7) + 4 * (vbo >> 5) + 2 * (l15 >> 3) + eo;
#pragma unroll
      for (int nb = 0; nb < 4; ++nb)
        out[t * 128 + 16 * cs + 4 * nb + g4] = v2u{pk(xo[4 * nb], xo[4 * nb + 1]), pk(xo[4 * nb + 2], xo[4 * nb + 3])};
      STAMP(29);
    }
  }
}

extern "C" void kernel_launch(void* const* d_in, const int* in_sizes, int n_in, void* d_out, int out_size, void* d_ws,
                              size_t ws_size, hipStream_t stream) {
  (void)in_sizes; (void)n_in; (void)out_size; (void)ws_size;
  (void)hipFuncSetAttribute((const void*)h3_mixer_kernel, hipFuncAttributeMaxDynamicSharedMemorySize, L_BYTES);
  h3_mixer_kernel<<<256, 256, L_BYTES, stream>>>(
      (const unsigned*)d_in[0], (const unsigned*)d_in[1], (const unsigned*)d_in[2], (const v4u*)d_in[3], (const v4u*)d_in[4],
      (const u16*)d_in[5], (const unsigned char*)d_in[7], (const u16*)d_in[8], (const unsigned char*)d_in[9], (const v2u*)d_in[10],
      (v2u*)d_out, (unsigned char*)d_ws);
}
```

```cpp
#define DIAG_K 99
#include <hip/hip_runtime.h>
#include <stdint.h>

#define GAS __attribute__((address_space(1)))
#define LAS __attribute__((address_space(3)))
typedef unsigned v4u __attribute__((ext_vector_type(4)));
typedef unsigned v2u __attribute__((ext_vector_type(2)));
typedef float f32x4 __attribute__((ext_vector_type(4)));
typedef float f32x2 __attribute__((ext_vector_type(2)));
typedef short s16x8 __attribute__((ext_vector_type(8)));
typedef short s16x4 __attribute__((ext_vector_type(4)));
typedef __bf16 bf16x2 __attribute__((ext_vector_type(2)));
typedef unsigned long long u64;
typedef GAS u64 gu64;
typedef unsigned short u16;

#define DBG_STAMP 0

namespace {
constexpr int O_E = 0;
constexpr int O_FA = 4096;
constexpr int O_FB = O_FA + 16384;
constexpr int O_C1 = O_FB + 16384;
constexpr int O_FX = O_C1 + 32768;
constexpr int O_ST1 = O_FX + 65536;
constexpr int O_B1 = O_ST1 + 16384;
constexpr int O_CS = O_B1 + 8192;
constexpr int O_FH = O_CS + 8192;
constexpr int O_ST2 = O_FH + 262144;
constexpr int ROWB = 4224;
constexpr int O_KVR = 1 << 20;
constexpr int O_QT = O_KVR + 512 * ROWB;
constexpr int O_W1T = O_QT + 512 * ROWB;
constexpr int O_W2T = O_W1T + 1835008;
constexpr int O_X1 = O_W2T + 1835008;
constexpr int O_Y = O_X1 + 2097152;
constexpr int O_X3 = O_Y + 2097152;
constexpr int O_H0 = O_X3 + 2097152;
constexpr int O_H1 = O_H0 + 7340032;
constexpr int O_END = O_H1 + 7340032;
constexpr int O_STAMP = O_END;
constexpr int L_W1 = 0, L_SCR = 114688, L_BYTES = 163840;
constexpr int L_BC = 111 * 1024;
constexpr int L_STG_T = 0, L_STG_W = 20480, L_CSP = 40960;
constexpr int HS_STR = 4128, L_HS = L_SCR, L_UR = L_SCR + 8 * HS_STR, UR_STR = 4576;
constexpr unsigned SPIN_CAP = 1u << 16;
constexpr float EPS = 1e-5f;

#define RLX_AGENT __ATOMIC_RELAXED, __HIP_MEMORY_SCOPE_AGENT
#define DRAIN() asm volatile("s_waitcnt vmcnt(0)" ::: "memory")
#define CFENCE() asm volatile("" ::: "memory")
#define SB() __builtin_amdgcn_sched_barrier(0)
#define BAR() do { asm volatile("s_waitcnt lgkmcnt(0)" ::: "memory"); __builtin_amdgcn_s_barrier(); CFENCE(); } while (0)
#define LD128(off) __builtin_amdgcn_raw_buffer_load_b128(rs, (off), 0, 16)
#define LD64(off) __builtin_amdgcn_raw_buffer_load_b64(rs, (off), 0, 16)
#define LD32(off) __builtin_amdgcn_raw_buffer_load_b32(rs, (off), 0, 16)
#define ST128(v, off) ST128S(v, off, 0, 0)
#define ST64(v, off) __builtin_amdgcn_raw_buffer_store_b64((v), rs, (off), 0, 16)
#define ST32(v, off) __builtin_amdgcn_raw_buffer_store_b32((v), rs, (off), 0, 16)
#define LD128S(vo, so, c) __builtin_amdgcn_raw_buffer_load_b128(rs, (vo) + ((c) & 4095), (so) + ((c) & ~4095), 16)
#define LD64S(vo, so, c) __builtin_amdgcn_raw_buffer_load_b64(rs, (vo) + ((c) & 4095), (so) + ((c) & ~4095), 16)
#define LD32S(vo, so, c) __builtin_amdgcn_raw_buffer_load_b32(rs, (vo) + ((c) & 4095), (so) + ((c) & ~4095), 16)
#define ST128S(v, vo, so, c) do { const v4u d_ = (v); __builtin_amdgcn_raw_buffer_store_b128(d_, rs, (vo) + ((c) & 4095), (so) + ((c) & ~4095), 16); \
                                  asm volatile("s_nop 1" :: "v"(d_)); } while (0)
#define ST64S(v, vo, so, c) __builtin_amdgcn_raw_buffer_store_b64((v), rs, (vo) + ((c) & 4095), (so) + ((c) & ~4095), 16)

__device__ __forceinline__ float lo16(unsigned x) { return __uint_as_float(x << 16); }
__device__ __forceinline__ float hi16(unsigned x) { return __uint_as_float(x & 0xffff0000u); }
__device__ __forceinline__ unsigned pk(float a, float b) {
  f32x2 v = {a, b};
  return __builtin_bit_cast(unsigned, __builtin_convertvector(v, bf16x2));
}
__device__ __forceinline__ float rbf(float a) { return lo16(pk(a, a)); }
__device__ __forceinline__ f32x4 mfma(v4u a, v4u b, f32x4 c) {
  return __builtin_amdgcn_mfma_f32_16x16x32_bf16(__builtin_bit_cast(s16x8, a), __builtin_bit_cast(s16x8, b), c, 0, 0, 0);
}
#define MF(acc, a, b) asm volatile("v_mfma_f32_16x16x32_bf16 %0, %1, %2, %0" : "+v"(acc) : "v"(a), "v"(b))
#define MF0(acc, a, b) asm volatile("v_mfma_f32_16x16x32_bf16 %0, %1, %2, 0" : "=&v"(acc) : "v"(a), "v"(b))
#define MFA(acc, a, b) asm volatile("v_mfma_f32_16x16x32_bf16 %0, %1, %2, %0" : "+v"(acc) : "a"(a), "v"(b))
#define MFA0(acc, a, b) asm volatile("v_mfma_f32_16x16x32_bf16 %0, %1, %2, 0" : "=&v"(acc) : "a"(a), "v"(b))
#define MFMA_BEGIN() do { SB(); asm volatile("s_nop 1"); SB(); } while (0)
#define MFMA_END() do { SB(); asm volatile("s_nop 7"); SB(); } while (0)
template <int C> __device__ __forceinline__ float dppf(float v) {
  return __uint_as_float(__builtin_amdgcn_update_dpp(0u, __float_as_uint(v), C, 0xf, 0xf, true));
}
__device__ __forceinline__ float sum16x(float v) {
  auto r = __builtin_amdgcn_permlane16_swap(__float_as_uint(v), __float_as_uint(v), false, false);
  return __uint_as_float(r[0]) + __uint_as_float(r[1]);
}
__device__ __forceinline__ float sum32x(float v) {
  auto r = __builtin_amdgcn_permlane32_swap(__float_as_uint(v), __float_as_uint(v), false, false);
  return __uint_as_float(r[0]) + __uint_as_float(r[1]);
}
__device__ __forceinline__ float wave_sum(float v) {
  v += dppf<0xB1>(v);
  v += dppf<0x4E>(v);
  v += dppf<0x141>(v);
  v += dppf<0x140>(v);
  return sum32x(sum16x(v));
}
__device__ __forceinline__ void wait_flags(gu64* p, bool act, u64 want) {
  for (unsigned s = 0; s < SPIN_CAP; ++s) {
    const u64 x = act ? __hip_atomic_load(p, RLX_AGENT) : want;
    if (__all(x == want)) break;
  }
  CFENCE();
}
__device__ __forceinline__ float gelu(float x) {
  const float z = x * __builtin_fmaf(x * x, -0.10294324f, -2.3022082f);
  return x * __builtin_amdgcn_rcpf(1.0f + __builtin_amdgcn_exp2f(z));
}

#define STAMP(i) do { if ((i) == 0) { diag0 = (unsigned)__builtin_amdgcn_s_memrealtime(); diagk = diag0; } else if ((i) == DIAG_K) { SB(); diagk = (unsigned)__builtin_amdgcn_s_memrealtime(); SB(); } } while (0)

struct Ctx {
  __amdgpu_buffer_rsrc_t rs;
  GAS unsigned char* ws;
  LAS unsigned char* lds;
  u64 tag;
  int lane, w, g, rh, jc, rq, cs;
};

}

__global__ void __launch_bounds__(256, 1)
h3_mixer_kernel(const unsigned* __restrict__ q, const unsigned* __restrict__ k, const unsigned* __restrict__ v,
                const v4u* __restrict__ sk, const v4u* __restrict__ hk, const u16* __restrict__ Dv,
                const unsigned char* __restrict__ w1, const u16* __restrict__ b1, const unsigned char* __restrict__ w2,
                const v2u* __restrict__ b2, v2u* __restrict__ out, unsigned char* wsp) {
  extern __shared__ __attribute__((aligned(16))) unsigned char lds_raw[];
  LAS unsigned char* lds = (LAS unsigned char*)lds_raw;
  GAS unsigned char* ws = (GAS unsigned char*)wsp;
  const __amdgpu_buffer_rsrc_t rs = __builtin_amdgcn_make_buffer_rsrc(wsp, 0, O_END, 0x00020000);
  const int b = blockIdx.x, tid = threadIdx.x, lane = tid & 63, w = __builtin_amdgcn_readfirstlane(tid >> 6);
  const int g = b & 7, j = b >> 3;
  const int l15 = lane & 15, g4 = lane >> 4;
  const int rh = j >> 4, jc = j & 15, rq = j >> 3, cs = j & 7;
  const int p = g * 32 + j;
  const int vl16 = lane * 16, vl8 = lane * 8;
  unsigned diag0, diagk;
  STAMP(0);

  const int cp = tid & 31, tq = tid >> 5;
  unsigned kk[11], vv[8], qq[8];
  {
    const int r0 = 64 * j + 8 * tq, col = 32 * g + cp;
#pragma unroll
    for (int i = 0; i < 11; ++i) {
      const int r = r0 - 3 + i;
      kk[i] = k[(r < 0 ? 0 : r) * 256 + col];
    }
#pragma unroll
    for (int i = 0; i < 8; ++i) vv[i] = v[(r0 + i) * 256 + col];
#pragma unroll
    for (int i = 0; i < 8; ++i) qq[i] = q[(r0 + i) * 256 + col];
    if (r0 == 0) kk[0] = kk[1] = kk[2] = 0;
  }
  const v4u tap = sk[32 * g + cp];
  const u64 e0 = __hip_atomic_load((gu64*)(ws + O_E), RLX_AGENT);
  unsigned touch = 0;
  SB();
  v4u qrow[2], hg[2][2], wraw[4];
#pragma unroll
  for (int i = 0; i < 2; ++i) qrow[i] = ((const v4u*)q)[(8 * b + 2 * w + i) * 64 + lane];
#pragma unroll
  for (int c = 0; c < 2; ++c) {
    hg[c][0] = hk[(2 * p + c) * 256 + (tid ? tid - 1 : 0)];
    hg[c][1] = hk[(2 * p + c) * 256 + tid];
  }
  int ksg = 0;
  if (b < 112) {
#pragma unroll
    for (int i = 0; i < 4; ++i)
      wraw[i] = *(const v4u*)(w1 + (32 * (4 * w + i) + (lane >> 1)) * 3584 + b * 32 + (lane & 1) * 16);
  } else {
    const int u = b < 224 ? b - 112 : 0;
    ksg = u >> 1;
    const int wq = ksg / 14, kl = ksg - 14 * wq;
    const int rowA = kl < 8 ? 112 * (4 * wq + (kl >> 1)) + 32 * (kl & 1)
                            : (kl < 12 ? 112 * (4 * wq + kl - 8) + 64 : 112 * (4 * wq + 2 * (kl - 12)) + 96);
    const int dB = kl < 12 ? 0 : 96;
#pragma unroll
    for (int i = 0; i < 4; ++i)
      wraw[i] = *(const v4u*)(w2 + (rowA + 8 * i + (lane >> 3) + (i >= 2 ? dB : 0)) * 1024 + (u & 1) * 512 + w * 128 + (lane & 7) * 16);
  }
  const float Dc0 = lo16(Dv[2 * p]), Dc1 = lo16(Dv[2 * p + 1]);
  SB();
  const u64 tag = ((u64)__builtin_amdgcn_readfirstlane((unsigned)(e0 >> 32)) << 32 | __builtin_amdgcn_readfirstlane((unsigned)e0)) +
                  0x9E3779B97F4A7C15ull;
  {
    const f32x2 t0 = {lo16(tap.x), lo16(tap.z)}, t1 = {hi16(tap.x), hi16(tap.z)}, t2 = {lo16(tap.y), lo16(tap.w)},
                t3 = {hi16(tap.y), hi16(tap.w)};
    f32x2 kf[11], r[8];
#pragma unroll
    for (int i = 0; i < 11; ++i) kf[i] = f32x2{lo16(kk[i]), hi16(kk[i])};
#pragma unroll
    for (int u = 0; u < 8; ++u)
      r[u] = (kf[u] * t3 + kf[u + 1] * t2 + kf[u + 2] * t1 + kf[u + 3] * t0) * f32x2{lo16(vv[u]), hi16(vv[u])};
    STAMP(1);
    LAS unsigned char* st = lds + L_STG_T + (2 * cp) * 144;
    *(LAS v4u*)(st + (7 - tq) * 16) = v4u{pk(r[7].x, r[6].x), pk(r[5].x, r[4].x), pk(r[3].x, r[2].x), pk(r[1].x, r[0].x)};
    *(LAS v4u*)(st + 144 + (7 - tq) * 16) = v4u{pk(r[7].y, r[6].y), pk(r[5].y, r[4].y), pk(r[3].y, r[2].y), pk(r[1].y, r[0].y)};
    *(LAS v4u*)(st + 9216 + tq * 16) =
        v4u{__builtin_amdgcn_perm(qq[1], qq[0], 0x05040100u), __builtin_amdgcn_perm(qq[3], qq[2], 0x05040100u),
            __builtin_amdgcn_perm(qq[5], qq[4], 0x05040100u), __builtin_amdgcn_perm(qq[7], qq[6], 0x05040100u)};
    *(LAS v4u*)(st + 9216 + 144 + tq * 16) =
        v4u{__builtin_amdgcn_perm(qq[1], qq[0], 0x07060302u), __builtin_amdgcn_perm(qq[3], qq[2], 0x07060302u),
            __builtin_amdgcn_perm(qq[5], qq[4], 0x07060302u), __builtin_amdgcn_perm(qq[7], qq[6], 0x07060302u)};
    BAR();
#pragma unroll
    for (int i = 0; i < 2; ++i) {
      const int ch = (tid >> 3) + 32 * i, pos = tid & 7;
      const v4u a = *(LAS v4u*)(lds + L_STG_T + ch * 144 + pos * 16);
      const v4u c = *(LAS v4u*)(lds + L_STG_T + 9216 + ch * 144 + pos * 16);
      ST128(a, O_KVR + (64 * g + ch) * ROWB + (248 - 8 * j + pos) * 16);
      ST128(c, O_QT + (64 * g + ch) * ROWB + (8 * j + pos) * 16);
    }
    if (j == 0 && tid < 32) __hip_atomic_store((gu64*)(ws + O_C1 + (tid * 8 + g) * 128), tag << 8, RLX_AGENT);
    STAMP(2);
    asm volatile("" :: "v"(touch));
    DRAIN();
    BAR();
    if (tid == 0) __hip_atomic_store((gu64*)(ws + O_FA + b * 64), tag, RLX_AGENT);
    STAMP(3);
  }
  {
    float ms[2][2];
#pragma unroll
    for (int i = 0; i < 2; ++i) {
      float x[8];
#pragma unroll
      for (int d = 0; d < 4; ++d) { x[2 * d] = lo16(qrow[i][d]); x[2 * d + 1] = hi16(qrow[i][d]); }
      float s = ((x[0] + x[1]) + (x[2] + x[3])) + ((x[4] + x[5]) + (x[6] + x[7]));
      const float m = wave_sum(s) * (1.0f / 512.0f);
      float sq = 0.f;
#pragma unroll
      for (int d = 0; d < 8; ++d) sq = __builtin_fmaf(x[d] - m, x[d] - m, sq);
      const float var = wave_sum(sq) * (1.0f / 512.0f);
      ms[i][0] = m;
      ms[i][1] = __builtin_sqrtf(rbf(rbf(var) + EPS));
    }
    if (lane == 0) ST128((v4u{pk(ms[0][0], ms[0][1]), pk(ms[1][0], ms[1][1]), (unsigned)tag, (unsigned)(tag >> 32)}), O_ST1 + (w * 256 + b) * 16);
    if (b < 224) {
#pragma unroll
      for (int i = 0; i < 4; ++i) *(LAS v4u*)(lds + L_STG_W + (4 * w + i) * 1024 + lane * 16) = wraw[i];
      const int tq4 = (lane >> 2) & 3, tp = lane & 3;
      if (b < 112) {
        const int jcp = b / 7, nbp = b - 7 * jcp;
        f32x4 cacc;
        v4u ones = {0x3F803F80u, 0x3F803F80u, 0x3F803F80u, 0x3F803F80u};
      asm volatile("" : "+v"(ones));
#pragma unroll
        for (int i = 0; i < 4; ++i) {
          LAS unsigned char* a = lds + L_STG_W + (4 * w + i) * 1024 + (4 * g4 + tq4) * 32 + tp * 8;
          const v2u x0 = __builtin_bit_cast(v2u, __builtin_amdgcn_ds_read_tr16_b64_v4i16((LAS s16x4*)a));
          const v2u x1 = __builtin_bit_cast(v2u, __builtin_amdgcn_ds_read_tr16_b64_v4i16((LAS s16x4*)(a + 512)));
          const v4u f = {x0.x, x0.y, x1.x, x1.y};
          ST128(f, O_W1T + ((jcp * 16 + 4 * w + i) * 7 + nbp) * 1024 + lane * 16);
          MFMA_BEGIN();
          if (i == 0) MF0(cacc, f, ones); else MF(cacc, f, ones);
        }
        MFMA_END();
        if (l15 == 0) *(LAS f32x4*)(lds + L_CSP + w * 64 + g4 * 16) = cacc;
      } else {
        const int csp = 4 * ((b - 112) & 1) + w;
#pragma unroll
        for (int nb = 0; nb < 4; ++nb) {
          LAS unsigned char* a = lds + L_STG_W + w * 4096 + (4 * g4 + tq4) * 128 + nb * 32 + tp * 8;
          const v2u x0 = __builtin_bit_cast(v2u, __builtin_amdgcn_ds_read_tr16_b64_v4i16((LAS s16x4*)a));
          const v2u x1 = __builtin_bit_cast(v2u, __builtin_amdgcn_ds_read_tr16_b64_v4i16((LAS s16x4*)(a + 2048)));
          ST128((v4u{x0.x, x0.y, x1.x, x1.y}), O_W2T + ((csp * 56 + ksg) * 4 + nb) * 1024 + lane * 16);
        }
      }
    }
#pragma unroll
    for (int c = 0; c < 2; ++c) {
      v4u a = hg[c][0];
      const v4u d = hg[c][1];
      if (tid == 0) a = v4u{0, 0, 0, 0};
      const unsigned s0 = __builtin_amdgcn_alignbyte(a.y, a.x, 2), s1 = __builtin_amdgcn_alignbyte(a.z, a.y, 2),
                     s2 = __builtin_amdgcn_alignbyte(a.w, a.z, 2), s3 = __builtin_amdgcn_alignbyte(d.x, a.w, 2),
                     s4 = __builtin_amdgcn_alignbyte(d.y, d.x, 2);
      LAS unsigned char* o = lds + L_HS + c * 4 * HS_STR + (tid + 1) * 16;
      *(LAS v4u*)(o) = v4u{s0, s1, s2, s3};
      *(LAS v4u*)(o + HS_STR) = v4u{a.y, a.z, a.w, d.x};
      *(LAS v4u*)(o + 2 * HS_STR) = v4u{s1, s2, s3, s4};
      *(LAS v4u*)(o + 3 * HS_STR) = v4u{a.z, a.w, d.x, d.y};
      if (tid == 255) {
        const unsigned u0 = __builtin_amdgcn_alignbyte(d.y, d.x, 2), u1 = __builtin_amdgcn_alignbyte(d.z, d.y, 2),
                       u2 = __builtin_amdgcn_alignbyte(d.w, d.z, 2), u3 = d.w >> 16;
        *(LAS v4u*)(o + 16) = v4u{u0, u1, u2, u3};
        *(LAS v4u*)(o + 16 + HS_STR) = v4u{d.y, d.z, d.w, 0};
        *(LAS v4u*)(o + 16 + 2 * HS_STR) = v4u{u1, u2, u3, 0};
        *(LAS v4u*)(o + 16 + 3 * HS_STR) = v4u{d.z, d.w, 0, 0};
      }
    }
    if (tid < 8) *(LAS v4u*)(lds + L_HS + tid * HS_STR) = v4u{0, 0, 0, 0};
    if (tid >= 64 && tid < 124) {
      const int i = tid - 64, c = i >= 30;
      *(LAS v4u*)(lds + L_UR + c * UR_STR + 4096 + (i - 30 * c) * 16) = v4u{0, 0, 0, 0};
    }
    STAMP(4);
    DRAIN();
    BAR();
    STAMP(5);
  }
  if (w == 0) {
    wait_flags((gu64*)(ws + O_FA + ((lane & 31) * 8 + g) * 64), lane < 32, tag);
    STAMP(6);
    v4u u[8];
#pragma unroll
    for (int i = 0; i < 8; ++i) u[i] = LD128S(vl16, O_KVR + (2 * p + (i >> 2)) * ROWB, (i & 3) * 1024);
#pragma unroll
    for (int i = 0; i < 8; ++i) *(LAS v4u*)(lds + L_UR + (i >> 2) * UR_STR + (i & 3) * 1024 + lane * 16) = u[i];
  } else {
    if (w == 1) {
      if (b < 112 && lane < 16) {
        const LAS float* cp4 = (const LAS float*)(lds + L_CSP);
        const float s = (cp4[lane] + cp4[16 + lane]) + (cp4[32 + lane] + cp4[48 + lane]);
        ST32(__float_as_uint(s), O_CS + (16 * b + lane) * 4);
        ST32((unsigned)b1[16 * b + lane] << 16, O_B1 + (16 * b + lane) * 4);
      }
      asm volatile("s_waitcnt vmcnt(0) lgkmcnt(0)" ::: "memory");
      if (lane == 0) __hip_atomic_store((gu64*)(ws + O_FB + b * 64), tag, RLX_AGENT);
    }
    wait_flags((gu64*)(ws + O_FB + (7 * jc + (lane & 7)) * 64), lane < 7, tag);
    STAMP(6);
  }
  BAR();
  STAMP(7);
  if (w != 0) {
    const unsigned voff = lane * 16;
    for (int gi = w - 1; gi < 28; gi += 3) {
      const u64 src = (u64)(uintptr_t)wsp + O_W1T + jc * 114688 + gi * 4096;
      const unsigned dst = (unsigned)(uintptr_t)lds + L_W1 + gi * 4096;
      unsigned keep;
      asm volatile(
          "s_mov_b32 %0, m0\n\ts_mov_b32 m0, %3\n\ts_nop 0\n\t"
          "global_load_lds_dwordx4 %1, %2 sc1\n\t"
          "global_load_lds_dwordx4 %1, %2 offset:1024 sc1\n\t"
          "global_load_lds_dwordx4 %1, %2 offset:2048 sc1\n\t"
          "global_load_lds_dwordx4 %1, %2 offset:3072 sc1\n\t"
          "s_mov_b32 m0, %0"
          : "=&s"(keep) : "v"(voff), "s"(src), "s"(dst) : "memory");
    }
  }
  v4u qg[2], sg[4];
#pragma unroll
  for (int c = 0; c < 2; ++c) qg[c] = LD128(O_QT + (2 * p + c) * ROWB + tid * 16);
#pragma unroll
  for (int i = 0; i < 4; ++i) sg[i] = LD128(O_ST1 + (i * 256 + tid) * 16);
  SB();
  {
    f32x4 acc[8];
    const int ch = w >> 1, rho0 = 4 * (w & 1);
    const LAS unsigned char* pa = lds + L_HS + (ch * 4 + (lane & 3)) * HS_STR + 8 * ((l15 >> 2) + 2 * g4) + 64 * rho0;
    const LAS unsigned char* pb = lds + L_UR + ch * UR_STR + 2 * (240 - 16 * l15 + 8 * g4) + 64 * rho0;
    v4u A[2][8], B[2][8];
#pragma unroll
    for (int s = 0; s < 8; ++s) {
      const v2u a0 = *(const LAS v2u*)(pa + 512 * s), a1 = *(const LAS v2u*)(pa + 512 * s + 8);
      A[0][s] = v4u{a0.x, a0.y, a1.x, a1.y};
      B[0][s] = *(const LAS v4u*)(pb + 512 * (7 - s));
    }
    MFMA_BEGIN();
#pragma unroll
    for (int r = 0; r < 4; ++r) {
      int n = 0;
#pragma unroll
      for (int sgm = 0; sgm < 8; ++sgm) {
#pragma unroll
        for (int dl = 0; dl + sgm < 8; ++dl) {
          if (r == 0 && sgm == 0) MF0(acc[dl], A[0][0], B[0][dl]);
          else MF(acc[sgm + dl], A[r & 1][sgm], B[r & 1][dl]);
          if (r < 3 && (n & 1) == 0 && (n >> 1) < 16) {
            const int s = n >> 2;
            if ((n & 2) == 0) {
              B[(r + 1) & 1][s] = *(const LAS v4u*)(pb + 64 * (r + 1) + 512 * (7 - s));
            } else {
              const v2u a0 = *(const LAS v2u*)(pa + 64 * (r + 1) + 512 * s), a1 = *(const LAS v2u*)(pa + 64 * (r + 1) + 512 * s + 8);
              A[(r + 1) & 1][s] = v4u{a0.x, a0.y, a1.x, a1.y};
            }
          }
          SB();
          ++n;
        }
      }
    }
    MFMA_END();
    STAMP(8);
    BAR();
    LAS unsigned char* rw = lds + L_SCR + w * 8192 + (g4 & 1) * 4096 + (2 * l15 + (g4 >> 1)) * 16;
#pragma unroll
    for (int c = 0; c < 8; ++c) *(LAS f32x4*)(rw + c * 512) = acc[c];
    BAR();
  }
  {
    for (unsigned s = 0; s < SPIN_CAP; ++s) {
      bool ok = true;
#pragma unroll
      for (int i = 0; i < 4; ++i) ok &= sg[i].z == (unsigned)tag && sg[i].w == (unsigned)(tag >> 32);
      if (__all(ok)) break;
      CFENCE();
#pragma unroll
      for (int i = 0; i < 4; ++i) sg[i] = LD128(O_ST1 + (i * 256 + tid) * 16);
    }
    float mu[8], ri[8];
#pragma unroll
    for (int i = 0; i < 4; ++i) {
      mu[2 * i] = lo16(sg[i].x); ri[2 * i] = __builtin_amdgcn_rcpf(hi16(sg[i].x));
      mu[2 * i + 1] = lo16(sg[i].y); ri[2 * i + 1] = __builtin_amdgcn_rcpf(hi16(sg[i].y));
    }
    float yv[2][8], xv[2][8];
#pragma unroll
    for (int c = 0; c < 2; ++c) {
      const v4u kr = *(const LAS v4u*)(lds + L_UR + c * UR_STR + (255 - tid) * 16);
      const float Dc = c ? Dc1 : Dc0;
#pragma unroll
      for (int hf = 0; hf < 2; ++hf) {
        const f32x4 y4 = *(const LAS f32x4*)(lds + L_SCR + (2 * c) * 8192 + hf * 4096 + tid * 16) +
                         *(const LAS f32x4*)(lds + L_SCR + (2 * c + 1) * 8192 + hf * 4096 + tid * 16);
#pragma unroll
        for (int i = 0; i < 4; ++i) {
          const int u = 4 * hf + i;
          const unsigned kd = kr[(7 - u) >> 1], qd = qg[c][u >> 1];
          const float kvv = (u & 1) ? lo16(kd) : hi16(kd);
          const float qv = (u & 1) ? hi16(qd) : lo16(qd);
          const float y = rbf(__builtin_fmaf(kvv, Dc, y4[i]));
          yv[c][u] = y;
          xv[c][u] = __builtin_fmaf((qv - mu[u]) * ri[u], y, qv);
        }
      }
    }
#pragma unroll
    for (int hf = 0; hf < 2; ++hf) {
      const int o = (hf * 32 + (tid >> 3)) * 32768 + p * 128 + (tid & 7) * 16;
      ST128((v4u{pk(xv[0][4 * hf], xv[1][4 * hf]), pk(xv[0][4 * hf + 1], xv[1][4 * hf + 1]), pk(xv[0][4 * hf + 2], xv[1][4 * hf + 2]),
                 pk(xv[0][4 * hf + 3], xv[1][4 * hf + 3])}), O_X1 + o);
      ST128((v4u{pk(yv[0][4 * hf], yv[1][4 * hf]), pk(yv[0][4 * hf + 1], yv[1][4 * hf + 1]), pk(yv[0][4 * hf + 2], yv[1][4 * hf + 2]),
                 pk(yv[0][4 * hf + 3], yv[1][4 * hf + 3])}), O_Y + o);
    }
    STAMP(9);
    DRAIN();
    BAR();
    STAMP(10);
  }
  const v4u wlast = *(const LAS v4u*)(lds + L_BC + lane * 16);
  const unsigned bcv = LD32((tid < 128 ? O_B1 : O_CS) + (112 * jc + ((tid & 127) < 112 ? (tid & 127) : 0)) * 4);
  if (tid < 32) __hip_atomic_fetch_add((gu64*)(ws + O_C1 + (tid * 8 + g) * 128), 1ull, RLX_AGENT);
  v4u w2r[14][4];
  wait_flags((gu64*)(ws + O_FB + (112 + 2 * (14 * w + (lane < 14 ? lane : 0)) + (cs >> 2)) * 64), lane < 14, tag);
#pragma unroll
  for (int i = 0; i < 14; ++i)
#pragma unroll
    for (int nb = 0; nb < 4; ++nb) w2r[i][nb] = LD128S(vl16, O_W2T + (cs * 56 + 14 * w) * 4096, (i * 4 + nb) * 1024);
  SB();
  asm volatile("" :: "a"(wlast));
  BAR();
  *(LAS unsigned*)(lds + L_BC + tid * 4) = bcv;
  if (w == 0) wait_flags((gu64*)(ws + O_C1 + ((b & 31) * 8 + (lane & 7)) * 128), lane < 8, (tag << 8) + 32);
  BAR();
  if (b == 0 && tid == 0) __hip_atomic_store((gu64*)(ws + O_E), tag, RLX_AGENT);
  STAMP(11);

  const int vb = 8 * g + 4 * rh + w;
  const int vbo = 8 * g + 2 * rq + (w >> 1), eo = w & 1;
  unsigned xres[8], yres[8];
  const LAS unsigned char* wl0 = lds + vl16;
  const LAS unsigned char* wl1 = lds + vl16 + 57344;
  asm volatile("" : "+v"(wl0), "+v"(wl1));
#define W1F(f) (*(const LAS v4u*)((f) < 56 ? wl0 + (f) * 1024 : wl1 + ((f) - 56) * 1024))
  v2u b2r[4];
#pragma unroll
  for (int nb = 0; nb < 4; ++nb) b2r[nb] = b2[16 * cs + 4 * nb + g4];

#pragma unroll
  for (int L = 0; L < 2; ++L) {
    const int OX = (L ? O_X3 : O_X1) + vb * 32768;
    const int OH = L ? O_H1 : O_H0;
    v4u F[16][2];
    f32x4 a1[4][2], aS[2], aQ[2];
    float rr[2], mr[2];
    {
      constexpr int WIN = 6;
      v4u raw[16][2];
#pragma unroll
      for (int ks = 0; ks < WIN; ++ks) { raw[ks][0] = LD128S(vl16, OX, ks * 2048); raw[ks][1] = LD128S(vl16, OX, ks * 2048 + 1024); }
      v4u ones = {0x3F803F80u, 0x3F803F80u, 0x3F803F80u, 0x3F803F80u};
      asm volatile("" : "+v"(ones));
      const bool hi = lane & 8;
      v4u W[16][4];
#pragma unroll
      for (int nb = 0; nb < 4; ++nb) W[0][nb] = W1F(nb);
#define SHUF(ks, e, i) do { if (L == 0) { const unsigned a_ = raw[ks][i][e], c_ = raw[ks][i][2 + (e)]; \
        const unsigned ar_ = __builtin_amdgcn_update_dpp(0u, a_, 0x128, 0xf, 0xf, true), cr_ = __builtin_amdgcn_update_dpp(0u, c_, 0x128, 0xf, 0xf, true); \
        F[ks][e][2 * (i)] = hi ? cr_ : a_; F[ks][e][2 * (i) + 1] = hi ? c_ : ar_; } \
        else if ((i) == 0) F[ks][e] = raw[ks][e]; } while (0)
      SHUF(0, 0, 0); SHUF(0, 0, 1); SHUF(0, 1, 0); SHUF(0, 1, 1);
      MFMA_BEGIN();
#pragma unroll
      for (int ks = 0; ks < 16; ++ks) {
#pragma unroll
        for (int n = 0; n < 12; ++n) {
          const int e = n & 1, nb = n >> 1;
          if (n < 8) { if (ks == 0) MF0(a1[nb][e], W[ks][nb], F[ks][e]); else MF(a1[nb][e], W[ks][nb], F[ks][e]); }
          else if (n < 10) { if (ks == 0) MF0(aS[e], ones, F[ks][e]); else MF(aS[e], ones, F[ks][e]); }
          else { if (ks == 0) MF0(aQ[e], F[ks][e], F[ks][e]); else MF(aQ[e], F[ks][e], F[ks][e]); }
          if (ks < 15 && n < 4) W[ks + 1][n] = W1F((ks + 1) * 7 + n);
          if (ks + WIN < 16 && n < 2) raw[ks + WIN][n] = LD128S(vl16, OX, (ks + WIN) * 2048 + n * 1024);
          if (ks < 15 && n >= 3 && n < 11 && (n & 1)) SHUF(ks + 1, ((n - 3) >> 1) & 1, (n - 3) >> 2);
          SB();
        }
      }
      MFMA_END();
    }
    STAMP(12 + 10 * L);
#pragma unroll
    for (int e = 0; e < 2; ++e) {
      const float d = (lane & 2) ? ((lane & 1) ? aQ[e][3] : aQ[e][2]) : ((lane & 1) ? aQ[e][1] : aQ[e][0]);
      const float sq = __uint_as_float(__builtin_amdgcn_ds_bpermute((l15 + 16 * (l15 >> 2)) * 4, __float_as_uint(d)));
      const float mu = aS[e][0] * (1.0f / 512.0f);
      const float var = __builtin_fmaxf(__builtin_fmaf(-mu, mu, sq * (1.0f / 512.0f)), 0.f);
      const float r1 = __builtin_amdgcn_rsqf(var + EPS);
      const float r = r1 * __builtin_amdgcn_rsqf(__builtin_fmaf(var * r1, r1, EPS));
      rr[e] = r;
      mr[e] = -mu * r;
    }
    const int hks = 14 * (jc >> 2), jq = jc & 3;
#define BV(nb) (*(const LAS f32x4*)(lds + L_BC + (16 * (nb) + 4 * g4) * 4))
#define CV(nb) (*(const LAS f32x4*)(lds + L_BC + 512 + (16 * (nb) + 4 * g4) * 4))
#define GELU_PAIR(acc, nb, e, i) pk(gelu(__builtin_fmaf(acc[i], rr[e], __builtin_fmaf(mr[e], CV(nb)[i], BV(nb)[i]))), \
                                   gelu(__builtin_fmaf(acc[(i) + 1], rr[e], __builtin_fmaf(mr[e], CV(nb)[(i) + 1], BV(nb)[(i) + 1]))))
#pragma unroll
    for (int e = 0; e < 2; ++e)
#pragma unroll
      for (int i = 0; i < 2; ++i) {
        const v4u o = {GELU_PAIR(a1[2 * i][e], 2 * i, e, 0), GELU_PAIR(a1[2 * i][e], 2 * i, e, 2),
                       GELU_PAIR(a1[2 * i + 1][e], 2 * i + 1, e, 0), GELU_PAIR(a1[2 * i + 1][e], 2 * i + 1, e, 2)};
        ST128S(o, vl16, OH + (vb * 112 + hks + 2 * jq) * 1024, (e * 56 + i) * 1024);
      }
    STAMP(13 + 10 * L);
    f32x4 a2[3][2];
    {
      v4u W[16][3];
#pragma unroll
      for (int ks = 0; ks < 2; ++ks)
#pragma unroll
        for (int nb = 0; nb < 3; ++nb) W[ks][nb] = W1F(ks * 7 + 4 + nb);
      MFMA_BEGIN();
#pragma unroll
      for (int ks = 0; ks < 16; ++ks) {
#pragma unroll
        for (int n = 0; n < 6; ++n) {
          const int e = n & 1, nb = n >> 1;
          if (ks == 0) MF0(a2[nb][e], W[ks][nb], F[ks][e]);
          else if (ks == 15 && nb == 2) MFA(a2[nb][e], wlast, F[ks][e]);
          else MF(a2[nb][e], W[ks][nb], F[ks][e]);
          if (ks < 14 && e == 0 && !(ks == 13 && nb == 2)) W[ks + 2][nb] = W1F((ks + 2) * 7 + 4 + nb);
          if (ks == 11 && n == 5) {
            DRAIN();
            if (lane == 0) __hip_atomic_store((gu64*)(ws + O_FH + (((L * 2) * 64 + vb) * 16 + jc) * 64), tag, RLX_AGENT);
          }
          SB();
        }
      }
      MFMA_END();
    }
    if (L == 0) {
      const int so = vbo * 32768 + cs * 4096 + eo * 4, vo = g4 * 256 + (l15 & 7) * 16 + (l15 >> 3) * 8;
#pragma unroll
      for (int i = 0; i < 8; ++i) {
        xres[i] = LD32S(vo, O_X1 + so, (i >> 1) * 1024 + (i & 1) * 128);
        yres[i] = LD32S(vo, O_Y + so, (i >> 1) * 1024 + (i & 1) * 128);
      }
    }
    STAMP(14 + 10 * L);
#pragma unroll
    for (int e = 0; e < 2; ++e) {
      const v4u o = {GELU_PAIR(a2[0][e], 4, e, 0), GELU_PAIR(a2[0][e], 4, e, 2), GELU_PAIR(a2[1][e], 5, e, 0), GELU_PAIR(a2[1][e], 5, e, 2)};
      ST128S(o, vl16, OH + (vb * 112 + hks + 8 + jq) * 1024, e * 56 * 1024);
      const v2u o2 = {GELU_PAIR(a2[2][e], 6, e, 0), GELU_PAIR(a2[2][e], 6, e, 2)};
      ST64S(o2, vl8, OH + (vb * 112 + hks + 12 + (jq >> 1)) * 1024 + (jq & 1) * 512, e * 56 * 1024);
    }
    STAMP(15 + 10 * L);
    gu64* fh = (gu64*)(ws + O_FH + (((L * 2) * 64 + 8 * g + 2 * rq + ((lane >> 2) & 1)) * 16 + 4 * w + (lane & 3)) * 64);
    wait_flags(fh, lane < 8, tag);
    DRAIN();
    if (lane == 0) __hip_atomic_store((gu64*)(ws + O_FH + (((L * 2 + 1) * 64 + vb) * 16 + jc) * 64), tag, RLX_AGENT);
    STAMP(16 + 10 * L);
    f32x4 acc[4][4];
    {
      int hb[4];
#pragma unroll
      for (int u = 0; u < 4; ++u) hb[u] = OH + ((16 * g + 4 * rq + ((w + u) & 3)) * 56 + 14 * w) * 1024;
      constexpr int WIN = 3;
      v4u hf[14][4];
#define LOAD_H(ks, u) do { if ((ks) < 12) hf[ks][u] = LD128S(vl16, hb[u], (ks) * 1024); \
        else { const v2u x0 = LD64S(vl8, hb[u], (ks) * 1024), x1 = LD64S(vl8, hb[u], (ks) * 1024 + 512); hf[ks][u] = v4u{x0.x, x0.y, x1.x, x1.y}; } } while (0)
#pragma unroll
      for (int ks = 0; ks < WIN; ++ks)
#pragma unroll
        for (int u = 0; u < 4; ++u) LOAD_H(ks, u);
      MFMA_BEGIN();
#pragma unroll
      for (int ks = 0; ks < 14; ++ks) {
        if (ks + WIN == 8) { wait_flags(fh + 64 * 16 * 8, lane < 8, tag); SB(); }
#pragma unroll
        for (int n = 0; n < 16; ++n) {
          const int u = n >> 2, nb = n & 3;
          if (ks == 0) MFA0(acc[u][nb], w2r[ks][nb], hf[ks][u]); else MFA(acc[u][nb], w2r[ks][nb], hf[ks][u]);
          if (ks + WIN < 14 && nb == 0) LOAD_H(ks + WIN, u);
          SB();
        }
      }
      MFMA_END();
    }
    STAMP(17 + 10 * L);
#pragma unroll
    for (int u = 1; u < 4; ++u)
#pragma unroll
      for (int nb = 0; nb < 4; ++nb)
        *(LAS f32x4*)(lds + L_SCR + (((w + u) & 3) * 3 + (u - 1)) * 4096 + nb * 1024 + lane * 16) = acc[u][nb];
    BAR();
    float xo[16];
#pragma unroll
    for (int nb = 0; nb < 4; ++nb) {
      f32x4 s = acc[0][nb];
#pragma unroll
      for (int u = 0; u < 3; ++u) s += *(const LAS f32x4*)(lds + L_SCR + (w * 3 + u) * 4096 + nb * 1024 + lane * 16);
      xo[4 * nb + 0] = s[0] + lo16(b2r[nb].x) + lo16(xres[2 * nb]);
      xo[4 * nb + 1] = s[1] + hi16(b2r[nb].x) + hi16(xres[2 * nb]);
      xo[4 * nb + 2] = s[2] + lo16(b2r[nb].y) + lo16(xres[2 * nb + 1]);
      xo[4 * nb + 3] = s[3] + hi16(b2r[nb].y) + hi16(xres[2 * nb + 1]);
    }
    STAMP(18 + 10 * L);
    if (L == 0) {
      float s = 0.f;
#pragma unroll
      for (int i = 0; i < 8; ++i) {
        const unsigned t = pk(xo[2 * i], xo[2 * i + 1]);
        xo[2 * i] = lo16(t);
        xo[2 * i + 1] = hi16(t);
        s += xo[2 * i] + xo[2 * i + 1];
      }
      const float mb = sum32x(sum16x(s)) * (1.0f / 64.0f);
      float m2 = 0.f;
#pragma unroll
      for (int i = 0; i < 16; ++i) m2 = __builtin_fmaf(xo[i] - mb, xo[i] - mb, m2);
      m2 = sum32x(sum16x(m2));
      const int so = O_ST2 + (vbo * 2 + eo) * 2048;
      if (lane < 16) ST128S((v4u{__float_as_uint(mb), __float_as_uint(m2), (unsigned)tag, (unsigned)(tag >> 32)}), vl16, so + cs * 256, 0);
      v4u s0, s1;
      for (unsigned it = 0; it < SPIN_CAP; ++it) {
        CFENCE();
        s0 = LD128S(vl16, so, 0);
        s1 = LD128S(vl16, so, 1024);
        if (__all(s0.z == (unsigned)tag && s0.w == (unsigned)(tag >> 32) && s1.z == (unsigned)tag && s1.w == (unsigned)(tag >> 32))) break;
      }
      STAMP(19);
      const float ma = __uint_as_float(s0.x), mc = __uint_as_float(s1.x);
      const float mu = sum32x(sum16x(ma + mc)) * 0.125f;
      const float da = ma - mu, dc = mc - mu;
      const float tot = sum32x(sum16x(__uint_as_float(s0.y) + __uint_as_float(s1.y) + 64.0f * (da * da + dc * dc)));
      const float r = __builtin_amdgcn_rsqf(tot * (1.0f / 512.0f) + EPS);
#pragma unroll
      for (int i = 0; i < 8; ++i)
        xres[i] = pk(__builtin_fmaf((xo[2 * i] - mu) * r, lo16(yres[i]), xo[2 * i]),
                     __builtin_fmaf((xo[2 * i + 1] - mu) * r, hi16(yres[i]), xo[2 * i + 1]));
#pragma unroll
      for (int i = 0; i < 2; ++i)
        ST128S((v4u{xres[4 * i], xres[4 * i + 1], xres[4 * i + 2], xres[4 * i + 3]}), vl16, O_X3 + vbo * 32768 + (4 * cs + eo) * 1024, i * 2048);
      DRAIN();
      if (lane == 0) __hip_atomic_store((gu64*)(ws + O_FX + ((vbo * 2 + eo) * 8 + cs) * 64), tag, RLX_AGENT);
      STAMP(20);
      wait_flags((gu64*)(ws + O_FX + (vb * 16 + l15) * 64), lane < 16, tag);
      STAMP(21);
    } else {
      const int t = 64 * (vbo & 31) + 8 * (l15 & 7) + 4 * (vbo >> 5) + 2 * (l15 >> 3) + eo;
#pragma unroll
      for (int nb = 0; nb < 4; ++nb)
        out[t * 128 + 16 * cs + 4 * nb + g4] = v2u{pk(xo[4 * nb], xo[4 * nb + 1]), pk(xo[4 * nb + 2], xo[4 * nb + 3])};
      STAMP(29);
    }
  }
  for (int i = 0; i < 100000; ++i) { if ((unsigned)__builtin_amdgcn_s_memrealtime() - diag0 >= 6000u + (diagk - diag0)) break; __builtin_amdgcn_s_sleep(1); }
}

extern "C" void kernel_launch(void* const* d_in, const int* in_sizes, int n_in, void* d_out, int out_size, void* d_ws,
                              size_t ws_size, hipStream_t stream) {
  (void)in_sizes; (void)n_in; (void)out_size; (void)ws_size;
  (void)hipFuncSetAttribute((const void*)h3_mixer_kernel, hipFuncAttributeMaxDynamicSharedMemorySize, L_BYTES);
  h3_mixer_kernel<<<256, 256, L_BYTES, stream>>>(
      (const unsigned*)d_in[0], (const unsigned*)d_in[1], (const unsigned*)d_in[2], (const v4u*)d_in[3], (const v4u*)d_in[4],
      (const u16*)d_in[5], (const unsigned char*)d_in[7], (const u16*)d_in[8], (const unsigned char*)d_in[9], (const v2u*)d_in[10],
      (v2u*)d_out, (unsigned char*)d_ws);
}
```

```cpp
#include <hip/hip_runtime.h>
#include <stdint.h>

#define GAS __attribute__((address_space(1)))
#define LAS __attribute__((address_space(3)))
typedef unsigned v4u __attribute__((ext_vector_type(4)));
typedef unsigned v2u __attribute__((ext_vector_type(2)));
typedef float f32x4 __attribute__((ext_vector_type(4)));
typedef float f32x2 __attribute__((ext_vector_type(2)));
typedef short s16x8 __attribute__((ext_vector_type(8)));
typedef short s16x4 __attribute__((ext_vector_type(4)));
typedef __bf16 bf16x2 __attribute__((ext_vector_type(2)));
typedef unsigned long long u64;
typedef GAS u64 gu64;
typedef unsigned short u16;

#define DBG_STAMP 0

#define NG1 12
#define VAL2 1
#define DR2 3
#define W2A 8
#define DR1 5
#define PL2 3
#define CK2 6
#define SLP_A 6
#define SLP_S1 14
#define SLP_ST 12
#define SLP_X3 12
#define CODE_PRE 45056
namespace {
constexpr int O_E = 0;
constexpr int O_FA = 4096;
constexpr int O_FB = O_FA + 65536;
constexpr int O_C1 = O_FB + 65536;
constexpr int O_FX = O_C1 + 32768;
constexpr int O_ST1 = O_FX + 65536;
constexpr int O_CS = O_ST1 + 16384;
constexpr int O_ST3 = O_CS + 32768;
constexpr int O_FH = O_ST3 + 131072;
constexpr int O_ST2 = O_FH + 262144;
constexpr int ROWB = 4224;
constexpr int O_KVR = 1 << 20;
constexpr int O_QT = O_KVR + 512 * ROWB;
constexpr int O_W1T = O_QT + 512 * ROWB;
constexpr int O_W2T = O_W1T + 1835008;
constexpr int O_X1 = O_W2T + 1835008;
constexpr int XS = 32768 + 128;
constexpr int O_Y = O_X1 + 64 * XS;
constexpr int O_X3 = O_Y + 64 * XS;
constexpr int O_H0 = O_X3 + 2097152;
constexpr int O_H1 = O_H0 + 7340032;
constexpr int O_END = O_H1 + 7340032;
constexpr int O_STAMP = O_END;
constexpr int L_W1 = 0, L_SCR = 114688, L_BYTES = 163840;
constexpr int L_BC = 111 * 1024;
constexpr int L_STG_T = 0, L_STG_W = 20480;
constexpr int HS_STR = 4128, L_HS = L_SCR, L_UR = L_SCR + 33792, UR_STR = 4576, RED_STR = 2112;
constexpr unsigned SPIN_CAP = 1u << 16;
constexpr float EPS = 1e-5f;

#define RLX_AGENT __ATOMIC_RELAXED, __HIP_MEMORY_SCOPE_AGENT
#define DRAIN() asm volatile("s_waitcnt vmcnt(0)" ::: "memory")
#define CFENCE() asm volatile("" ::: "memory")
#define SB() __builtin_amdgcn_sched_barrier(0)
#define BAR() do { asm volatile("s_waitcnt lgkmcnt(0)" ::: "memory"); __builtin_amdgcn_s_barrier(); CFENCE(); } while (0)
#define LD128(off) __builtin_amdgcn_raw_buffer_load_b128(rs, (off), 0, 16)
#define LD64(off) __builtin_amdgcn_raw_buffer_load_b64(rs, (off), 0, 16)
#define LD32(off) __builtin_amdgcn_raw_buffer_load_b32(rs, (off), 0, 16)
#define ST128(v, off) ST128S(v, off, 0, 0)
#define ST64(v, off) __builtin_amdgcn_raw_buffer_store_b64((v), rs, (off), 0, 16)
#define ST32(v, off) __builtin_amdgcn_raw_buffer_store_b32((v), rs, (off), 0, 16)
#define LD128S(vo, so, c) __builtin_amdgcn_raw_buffer_load_b128(rs, (vo) + ((c) & 4095), (so) + ((c) & ~4095), 16)
#define LD64S(vo, so, c) __builtin_amdgcn_raw_buffer_load_b64(rs, (vo) + ((c) & 4095), (so) + ((c) & ~4095), 16)
#define LD32S(vo, so, c) __builtin_amdgcn_raw_buffer_load_b32(rs, (vo) + ((c) & 4095), (so) + ((c) & ~4095), 16)
#define ST128S(v, vo, so, c) do { const v4u d_ = (v); __builtin_amdgcn_raw_buffer_store_b128(d_, rs, (vo) + ((c) & 4095), (so) + ((c) & ~4095), 16); \
                                  asm volatile("s_nop 1" :: "v"(d_)); } while (0)
#define ST64S(v, vo, so, c) __builtin_amdgcn_raw_buffer_store_b64((v), rs, (vo) + ((c) & 4095), (so) + ((c) & ~4095), 16)

__device__ __forceinline__ float lo16(unsigned x) { return __uint_as_float(x << 16); }
__device__ __forceinline__ float hi16(unsigned x) { return __uint_as_float(x & 0xffff0000u); }
__device__ __forceinline__ unsigned pk(float a, float b) {
  f32x2 v = {a, b};
  return __builtin_bit_cast(unsigned, __builtin_convertvector(v, bf16x2));
}
__device__ __forceinline__ float rbf(float a) { return lo16(pk(a, a)); }
__device__ __forceinline__ f32x4 mfma(v4u a, v4u b, f32x4 c) {
  return __builtin_amdgcn_mfma_f32_16x16x32_bf16(__builtin_bit_cast(s16x8, a), __builtin_bit_cast(s16x8, b), c, 0, 0, 0);
}
#define MF(acc, a, b) asm volatile("v_mfma_f32_16x16x32_bf16 %0, %1, %2, %0" : "+v"(acc) : "v"(a), "v"(b))
#define MF0(acc, a, b) asm volatile("v_mfma_f32_16x16x32_bf16 %0, %1, %2, 0" : "=&v"(acc) : "v"(a), "v"(b))
#define MFA(acc, a, b) asm volatile("v_mfma_f32_16x16x32_bf16 %0, %1, %2, %0" : "+v"(acc) : "a"(a), "v"(b))
#define MFA0(acc, a, b) asm volatile("v_mfma_f32_16x16x32_bf16 %0, %1, %2, 0" : "=&v"(acc) : "a"(a), "v"(b))
#define MFMA_BEGIN() do { SB(); asm volatile("s_nop 1"); SB(); } while (0)
#define MFMA_END() do { SB(); asm volatile("s_nop 7"); SB(); } while (0)
template <int C> __device__ __forceinline__ float dppf(float v) {
  return __uint_as_float(__builtin_amdgcn_update_dpp(0u, __float_as_uint(v), C, 0xf, 0xf, true));
}
__device__ __forceinline__ float sum16x(float v) {
  auto r = __builtin_amdgcn_permlane16_swap(__float_as_uint(v), __float_as_uint(v), false, false);
  return __uint_as_float(r[0]) + __uint_as_float(r[1]);
}
__device__ __forceinline__ float sum32x(float v) {
  auto r = __builtin_amdgcn_permlane32_swap(__float_as_uint(v), __float_as_uint(v), false, false);
  return __uint_as_float(r[0]) + __uint_as_float(r[1]);
}
__device__ __forceinline__ float wave_sum(float v) {
  v += dppf<0xB1>(v);
  v += dppf<0x4E>(v);
  v += dppf<0x141>(v);
  v += dppf<0x140>(v);
  return sum32x(sum16x(v));
}
__device__ __forceinline__ void wait_flags(gu64* p, u64 want) {
#pragma unroll 1
  for (unsigned s = 0; s < SPIN_CAP; ++s) {
    const u64 x = __hip_atomic_load(p, RLX_AGENT);
    if (__all(x == want)) break;
  }
  __builtin_amdgcn_s_waitcnt(0x0F70);
  CFENCE();
}
__device__ __forceinline__ unsigned san(unsigned x) { return x < 0xFFFFFFFEu ? x : 0xFFFFFFFEu; }
__device__ __forceinline__ unsigned umax3(unsigned a, unsigned b, unsigned c) { const unsigned m = a > b ? a : b; return m > c ? m : c; }
__device__ __forceinline__ f32x2 lo2(f32x4 v) { return f32x2{v[0], v[1]}; }
__device__ __forceinline__ f32x2 hi2(f32x4 v) { return f32x2{v[2], v[3]}; }
template <int N, bool SAN>
__device__ __forceinline__ void gelu_pairs(const f32x2 (&acc)[N], const f32x2 (&bb)[N], const f32x2 (&cc)[N], float r, float mr,
                                           f32x2 k1, f32x2 k3, unsigned (&o)[N]) {
  const f32x2 r2 = {r, r}, m2 = {mr, mr};
  f32x2 u[N], z[N];
#pragma unroll
  for (int i = 0; i < N; ++i) u[i] = __builtin_elementwise_fma(m2, cc[i], bb[i]);
#pragma unroll
  for (int i = 0; i < N; ++i) u[i] = __builtin_elementwise_fma(acc[i], r2, u[i]);
#pragma unroll
  for (int i = 0; i < N; ++i) z[i] = __builtin_elementwise_fma(u[i] * u[i], k3, k1) * u[i];
  SB();
#pragma unroll
  for (int i = 0; i < N; ++i) z[i] = f32x2{__builtin_amdgcn_exp2f(z[i].x), __builtin_amdgcn_exp2f(z[i].y)};
  SB();
#pragma unroll
  for (int i = 0; i < N; ++i) z[i] += 1.0f;
  SB();
#pragma unroll
  for (int i = 0; i < N; ++i) z[i] = f32x2{__builtin_amdgcn_rcpf(z[i].x), __builtin_amdgcn_rcpf(z[i].y)};
  SB();
#pragma unroll
  for (int i = 0; i < N; ++i) { const f32x2 y = u[i] * z[i]; o[i] = SAN ? san(pk(y.x, y.y)) : pk(y.x, y.y); }
}

#define STAMP(i) do { } while (0)

template <int V> struct IC { static constexpr int value = V; };
#define POST(off) do { if (lane == 0) __hip_atomic_store((gu64*)(ws + (off)), tag, RLX_AGENT); } while (0)
#define LDA128(dst, so, c) asm volatile("buffer_load_dwordx4 %0, %1, %2, %3 offen offset:%4 sc1" : "=a"(dst) \
                                        : "v"(vl16), "s"(rsd), "s"((so) + ((c) & ~4095)), "i"((c) & 4095) : "memory")

}

__global__ void __launch_bounds__(256, 1)
h3_mixer_kernel(const unsigned* __restrict__ q, const unsigned* __restrict__ k, const unsigned* __restrict__ v,
                const v4u* __restrict__ sk, const v4u* __restrict__ hk, const unsigned* __restrict__ Dv,
                const unsigned char* __restrict__ w1, const u16* __restrict__ b1, const unsigned char* __restrict__ w2,
                const v2u* __restrict__ b2, unsigned char* __restrict__ out, unsigned char* wsp) {
  extern __shared__ __attribute__((aligned(16))) unsigned char lds_raw[];
  LAS unsigned char* lds = (LAS unsigned char*)lds_raw;
  GAS unsigned char* ws = (GAS unsigned char*)wsp;
  const __amdgpu_buffer_rsrc_t rs = __builtin_amdgcn_make_buffer_rsrc(wsp, 0, O_END, 0x00020000);
  const v4u rsd = {(unsigned)(uintptr_t)wsp, (unsigned)((uintptr_t)wsp >> 32) & 0xffffu, (unsigned)O_END, 0x00020000u};
  const int b = blockIdx.x, tid = threadIdx.x, lane = tid & 63, w = __builtin_amdgcn_readfirstlane(tid >> 6);
  const int g = b & 7, j = b >> 3;
  const int l15 = lane & 15, g4 = lane >> 4;
  const int rh = j >> 4, jc = j & 15, rq = j >> 3, cs = j & 7;
  const int p = g * 32 + j;
  const int vl16 = lane * 16, vl8 = lane * 8;
  STAMP(0);

  const int cp = tid & 31, tq = tid >> 5;
  unsigned kk[11], vv[8], qq[8];
  const int r0 = 64 * j + 8 * tq, col = 32 * g + cp;
#pragma unroll
  for (int i = 0; i < 11; ++i) {
    const int r = r0 - 3 + i;
    kk[i] = k[(r < 0 ? 0 : r) * 256 + col];
  }
#pragma unroll
  for (int i = 0; i < 8; ++i) vv[i] = v[(r0 + i) * 256 + col];
  const v4u tap = sk[col];
  const u64 e0 = __hip_atomic_load((gu64*)(ws + O_E), RLX_AGENT);
#pragma unroll
  for (int i = 0; i < 8; ++i) qq[i] = q[(r0 + i) * 256 + col];
  SB();
  v4u qrow[2], hg[2][2], wraw[4];
  int ksg = 0;
  {
    const unsigned char* src;
    int step, dB = 0;
    if (b < 112) {
      src = w1 + (128 * w + (lane >> 1)) * 3584 + b * 32 + (lane & 1) * 16;
      step = 32 * 3584;
    } else {
      const int u = b < 224 ? b - 112 : 0;
      ksg = u >> 1;
      const int wq = ksg / 14, kl = ksg - 14 * wq;
      const int rowA = kl < 8 ? 112 * (4 * wq + (kl >> 1)) + 32 * (kl & 1)
                              : (kl < 12 ? 112 * (4 * wq + kl - 8) + 64 : 112 * (4 * wq + 2 * (kl - 12)) + 96);
      dB = kl < 12 ? 0 : 96 * 1024;
      src = w2 + (rowA + (lane >> 3)) * 1024 + (u & 1) * 512 + w * 128 + (lane & 7) * 16;
      step = 8 * 1024;
    }
#pragma unroll
    for (int i = 0; i < 4; ++i) wraw[i] = *(const v4u*)(src + i * step + (i >= 2 ? dB : 0));
  }
#pragma unroll
  for (int c = 0; c < 2; ++c) {
    hg[c][0] = hk[(2 * p + c) * 256 + (tid ? tid - 1 : 0)];
    hg[c][1] = hk[(2 * p + c) * 256 + tid];
  }
#pragma unroll
  for (int i = 0; i < 2; ++i) qrow[i] = ((const v4u*)q)[(8 * b + 2 * w + i) * 64 + lane];
  const unsigned Dpair = Dv[p];
  const unsigned b1v = b1[112 * jc + (tid < 112 ? tid : 0)];
  unsigned pre = 0;
  {
    u64 pc;
    asm volatile("s_getpc_b64 %0" : "=s"(pc));
    if (tid < CODE_PRE / 512) pre = *(const GAS unsigned*)((pc & ~15ull) + j * (CODE_PRE / 32) + tid * 16);
  }
  const int vb = 8 * g + 4 * rh + w;
  const int vbo = 8 * g + 2 * rq + (w >> 1), eo = w & 1;
  {
    const int bw = b * 4 + w, to = vbo * 2 + eo;
    const int o = l15 < 2 ? O_FA + l15 * 65536 + bw * 64
                : l15 < 6 ? O_FH + (((l15 - 2) * 64 + vb) * 16 + jc) * 64
                : l15 < 7 ? O_FX + (to * 8 + cs) * 64
                : l15 < 9 ? O_ST2 + to * 2048 + cs * 256 + (l15 - 7) * 128
                : l15 < 10 ? O_ST1 + (w * 256 + b) * 16
                : O_C1 + b * 128;
    pre += LD32(o);
  }
  SB();
  if (r0 == 0) kk[0] = kk[1] = kk[2] = 0;
  const u64 tag = ((u64)__builtin_amdgcn_readfirstlane((unsigned)(e0 >> 32)) << 32 | __builtin_amdgcn_readfirstlane((unsigned)e0)) +
                  0x9E3779B97F4A7C15ull;
  {
    const f32x2 t0 = {lo16(tap.x), lo16(tap.z)}, t1 = {hi16(tap.x), hi16(tap.z)}, t2 = {lo16(tap.y), lo16(tap.w)},
                t3 = {hi16(tap.y), hi16(tap.w)};
    f32x2 kf[11], r[8];
#pragma unroll
    for (int i = 0; i < 11; ++i) kf[i] = f32x2{lo16(kk[i]), hi16(kk[i])};
#pragma unroll
    for (int u = 0; u < 8; ++u)
      r[u] = (kf[u] * t3 + kf[u + 1] * t2 + kf[u + 2] * t1 + kf[u + 3] * t0) * f32x2{lo16(vv[u]), hi16(vv[u])};
    STAMP(1);
    LAS unsigned char* st = lds + L_STG_T + (2 * cp) * 144;
    *(LAS v4u*)(st + (7 - tq) * 16) = v4u{pk(r[7].x, r[6].x), pk(r[5].x, r[4].x), pk(r[3].x, r[2].x), pk(r[1].x, r[0].x)};
    *(LAS v4u*)(st + 144 + (7 - tq) * 16) = v4u{pk(r[7].y, r[6].y), pk(r[5].y, r[4].y), pk(r[3].y, r[2].y), pk(r[1].y, r[0].y)};
    *(LAS v4u*)(st + 9216 + tq * 16) =
        v4u{__builtin_amdgcn_perm(qq[1], qq[0], 0x05040100u), __builtin_amdgcn_perm(qq[3], qq[2], 0x05040100u),
            __builtin_amdgcn_perm(qq[5], qq[4], 0x05040100u), __builtin_amdgcn_perm(qq[7], qq[6], 0x05040100u)};
    *(LAS v4u*)(st + 9216 + 144 + tq * 16) =
        v4u{__builtin_amdgcn_perm(qq[1], qq[0], 0x07060302u), __builtin_amdgcn_perm(qq[3], qq[2], 0x07060302u),
            __builtin_amdgcn_perm(qq[5], qq[4], 0x07060302u), __builtin_amdgcn_perm(qq[7], qq[6], 0x07060302u)};
    BAR();
    v4u ta[2], tc[2];
#pragma unroll
    for (int i = 0; i < 2; ++i) {
      ta[i] = *(LAS v4u*)(lds + L_STG_T + ((tid >> 3) + 32 * i) * 144 + (tid & 7) * 16);
      tc[i] = *(LAS v4u*)(lds + L_STG_T + 9216 + ((tid >> 3) + 32 * i) * 144 + (tid & 7) * 16);
    }
    const int vo = (tid >> 3) * ROWB + (tid & 7) * 16;
#pragma unroll
    for (int i = 0; i < 2; ++i) ST128S(ta[i], vo, O_KVR + (64 * g + 32 * i) * ROWB + (248 - 8 * j) * 16, 0);
#pragma unroll
    for (int i = 0; i < 2; ++i) ST128S(tc[i], vo, O_QT + (64 * g + 32 * i) * ROWB + 8 * j * 16, 0);
    STAMP(2);
  }
  {
#pragma unroll
    for (int i = 0; i < 4; ++i) *(LAS v4u*)(lds + L_STG_W + (4 * w + i) * 1024 + vl16) = wraw[i];
    const int tq4 = (lane >> 2) & 3, tp = lane & 3;
    const bool is1 = b < 112;
    LAS unsigned char* a = lds + L_STG_W + w * 4096 + (4 * g4 + tq4) * (is1 ? 32 : 128) + tp * 8;
    const int sI = is1 ? 1024 : 32, sH = is1 ? 512 : 2048;
    v4u f[4];
#pragma unroll
    for (int i = 0; i < 4; ++i) {
      const v2u x0 = __builtin_bit_cast(v2u, __builtin_amdgcn_ds_read_tr16_b64_v4i16((LAS s16x4*)(a + i * sI)));
      const v2u x1 = __builtin_bit_cast(v2u, __builtin_amdgcn_ds_read_tr16_b64_v4i16((LAS s16x4*)(a + i * sI + sH)));
      f[i] = v4u{x0.x, x0.y, x1.x, x1.y};
    }
    const int jcp = b / 7, nbp = b - 7 * jcp;
    const int so = is1 ? O_W1T + ((jcp * 16 + 4 * w) * 7 + nbp) * 1024
                       : (b < 224 ? O_W2T + ((4 * ((b - 112) & 1) + w) * 56 + ksg) * 4096 : O_END);
    const int ss = is1 ? 7168 : 1024;
#pragma unroll
    for (int i = 0; i < 4; ++i) ST128S(f[i], vl16, so + i * ss, 0);
    v4u ones = {0x3F803F80u, 0x3F803F80u, 0x3F803F80u, 0x3F803F80u};
    asm volatile("" : "+v"(ones));
    f32x4 cacc;
    MFMA_BEGIN();
    MF0(cacc, f[0], ones);
    MF(cacc, f[1], ones);
    MF(cacc, f[2], ones);
    MF(cacc, f[3], ones);
    MFMA_END();
    const bool ini = (b >> 3) == 28 && w == 0;
    const v4u d5 = ini ? v4u{(unsigned)(tag << 8), (unsigned)(tag >> 24), 0u, 0u} : __builtin_bit_cast(v4u, cacc);
    const int vo5 = ini ? (lane < 32 ? (lane * 8 + g) * 128 : O_END) : (is1 && l15 == 0 ? g4 * 16 : O_END);
    ST128S(d5, vo5, ini ? O_C1 : O_CS + w * 8192 + b * 64, 0);
    asm volatile("s_waitcnt vmcnt(5)" :: "v"(pre) : "memory");
    POST(O_FA + (b * 4 + w) * 64);
    STAMP(3);
  }
  const v4u ONES4 = {~0u, ~0u, ~0u, ~0u};
  const int vox = (tid >> 3) * XS + (tid & 7) * 16;
  const int hks = 14 * (jc >> 2), jq = jc & 3;
#define FILL_H(OH, i) do { const int e_ = (i) & 1; \
    if ((i) < 2) ST128S(ONES4, vl16, (OH) + (vb * 112 + hks + 8 + jq) * 1024, e_ * 56 * 1024); \
    else ST64S((v2u{~0u, ~0u}), vl8, (OH) + (vb * 112 + hks + 12 + (jq >> 1)) * 1024 + (jq & 1) * 512, e_ * 56 * 1024); } while (0)
  {
#pragma unroll
    for (int c = 0; c < 2; ++c) {
      v4u a = hg[c][0];
      const v4u d = hg[c][1];
      if (tid == 0) a = v4u{0, 0, 0, 0};
      const unsigned s0 = __builtin_amdgcn_alignbyte(a.y, a.x, 2), s1 = __builtin_amdgcn_alignbyte(a.z, a.y, 2),
                     s2 = __builtin_amdgcn_alignbyte(a.w, a.z, 2), s3 = __builtin_amdgcn_alignbyte(d.x, a.w, 2),
                     s4 = __builtin_amdgcn_alignbyte(d.y, d.x, 2);
      LAS unsigned char* o = lds + L_HS + c * 4 * HS_STR + (tid + 1) * 16;
      *(LAS v4u*)(o) = v4u{s0, s1, s2, s3};
      *(LAS v4u*)(o + HS_STR) = v4u{a.y, a.z, a.w, d.x};
      *(LAS v4u*)(o + 2 * HS_STR) = v4u{s1, s2, s3, s4};
      *(LAS v4u*)(o + 3 * HS_STR) = v4u{a.z, a.w, d.x, d.y};
      if (tid == 255) {
        const unsigned u1 = __builtin_amdgcn_alignbyte(d.z, d.y, 2), u2 = __builtin_amdgcn_alignbyte(d.w, d.z, 2), u3 = d.w >> 16;
        *(LAS v4u*)(o + 16) = v4u{s4, u1, u2, u3};
        *(LAS v4u*)(o + 16 + HS_STR) = v4u{d.y, d.z, d.w, 0};
        *(LAS v4u*)(o + 16 + 2 * HS_STR) = v4u{u1, u2, u3, 0};
        *(LAS v4u*)(o + 16 + 3 * HS_STR) = v4u{d.z, d.w, 0, 0};
      }
    }
    if (tid < 68) {
      const int i = tid - 8, c = i >= 30;
      *(LAS v4u*)(lds + (tid < 8 ? L_HS + tid * HS_STR : L_UR + c * UR_STR + 4096 + (i - 30 * c) * 16)) = v4u{0, 0, 0, 0};
    }
    asm volatile("s_waitcnt vmcnt(1)" ::: "memory");
    POST(O_FB + (b * 4 + w) * 64);
    float ms[2][2];
#pragma unroll
    for (int i = 0; i < 2; ++i) {
      f32x2 x[4];
#pragma unroll
      for (int d = 0; d < 4; ++d) x[d] = f32x2{lo16(qrow[i][d]), hi16(qrow[i][d])};
      const f32x2 s = (x[0] + x[1]) + (x[2] + x[3]);
      const float m = wave_sum(s.x + s.y) * (1.0f / 512.0f);
      f32x2 sq = {0.f, 0.f};
#pragma unroll
      for (int d = 0; d < 4; ++d) { const f32x2 t = x[d] - m; sq = __builtin_elementwise_fma(t, t, sq); }
      const float var = wave_sum(sq.x + sq.y) * (1.0f / 512.0f);
      ms[i][0] = m;
      ms[i][1] = __builtin_amdgcn_sqrtf(rbf(rbf(var) + EPS));
    }
    if (lane == 0) ST128S((v4u{pk(ms[0][0], ms[0][1]), pk(ms[1][0], ms[1][1]), (unsigned)tag, (unsigned)(tag >> 32)}), 0, O_ST1 + (w * 256 + b) * 16, 0);
    STAMP(4);
  }
  if (SLP_A) __builtin_amdgcn_s_sleep(SLP_A);
  wait_flags((gu64*)(ws + O_FA + (((lane & 31) * 8 + g) * 4 + ((j >> 2) & 3)) * 64), tag);
  STAMP(5);
  v4u qg[2], sg[4];
  gu64* fb1 = (gu64*)(ws + O_FB + (lane < 28 ? 28 * jc + lane : 4 * (224 + g)) * 64);
  {
    v4u u[2];
#pragma unroll
    for (int c = 0; c < 2; ++c) u[c] = LD128S(vl16, O_KVR + (2 * p + c) * ROWB + w * 1024, 0);
#pragma unroll
    for (int c = 0; c < 2; ++c) qg[c] = LD128S(vl16, O_QT + (2 * p + c) * ROWB + w * 1024, 0);
    const u64 fbv = __hip_atomic_load(fb1, RLX_AGENT);
    SB();
#pragma unroll
    for (int c = 0; c < 2; ++c) *(LAS v4u*)(lds + L_UR + c * UR_STR + w * 1024 + vl16) = u[c];
    if (__builtin_expect(!__all(fbv == tag), 0)) wait_flags(fb1, tag);
    asm volatile("" :: "v"(qg[0]), "v"(qg[1]) : "memory");
  }
  BAR();
  STAMP(7);
  v4u wlast;
  float bcv;
  {
    f32x4 acc[8];
    const int ch = w >> 1, rho0 = 4 * (w & 1);
    const LAS unsigned char* pa = lds + L_HS + (ch * 4 + (lane & 3)) * HS_STR + 8 * ((l15 >> 2) + 2 * g4) + 64 * rho0;
    const LAS unsigned char* pb = lds + L_UR + ch * UR_STR + 2 * (240 - 16 * l15 + 8 * g4) + 64 * rho0;
    const u64 gsrc = (u64)(uintptr_t)wsp + O_W1T + jc * 114688 + w * (NG1 * 1024);
    const unsigned gdst = (unsigned)(uintptr_t)lds + L_W1 + w * (NG1 * 1024);
    unsigned csp[4];
    LDA128(wlast, O_W1T + jc * 114688, 111 * 1024);
    v4u A[2][8], B[2][8];
#pragma unroll
    for (int s = 0; s < 8; ++s) {
      B[0][s] = *(const LAS v4u*)(pb + 512 * (7 - s));
      const v2u a0 = *(const LAS v2u*)(pa + 512 * s), a1 = *(const LAS v2u*)(pa + 512 * s + 8);
      A[0][s] = v4u{a0.x, a0.y, a1.x, a1.y};
    }
    MFMA_BEGIN();
#pragma unroll
    for (int r = 0; r < 4; ++r) {
      int n = 0;
      if (r == 3) {
#pragma unroll
        for (int i = 0; i < 4; ++i) sg[i] = LD128S(vl16, O_ST1 + w * 1024, i * 4096);
#pragma unroll
        for (int i = 0; i < 4; ++i) csp[i] = LD32S((tid < 112 ? tid : 0) * 4, O_CS + 448 * jc, i * 8192);
        SB();
      }
#pragma unroll
      for (int sgm = 0; sgm < 8; ++sgm) {
#pragma unroll
        for (int dl = 0; dl + sgm < 8; ++dl) {
          if (r == 0 && sgm == 0) MF0(acc[dl], A[0][0], B[0][dl]);
          else MF(acc[sgm + dl], A[r & 1][sgm], B[r & 1][dl]);
          if (r < 3 && (n & 1) == 0 && (n >> 1) < 16) {
            const int s = n >> 2;
            if ((n & 2) == 0) {
              B[(r + 1) & 1][s] = *(const LAS v4u*)(pb + 64 * (r + 1) + 512 * (7 - s));
            } else {
              const v2u a0 = *(const LAS v2u*)(pa + 64 * (r + 1) + 512 * s), a1 = *(const LAS v2u*)(pa + 64 * (r + 1) + 512 * s + 8);
              A[(r + 1) & 1][s] = v4u{a0.x, a0.y, a1.x, a1.y};
            }
          }
          if (NG1 && r < 3 && n % (108 / (NG1 ? NG1 : 1)) == 1 && n / (108 / (NG1 ? NG1 : 1)) < NG1 / 3) {
            const int c = r * (NG1 / 3) + n / (108 / (NG1 ? NG1 : 1));
              asm volatile("s_mov_b32 m0, %2\n\ts_nop 0\n\tglobal_load_lds_dwordx4 %0, %1 offset:%3 sc1"
                           :: "v"(vl16), "s"(gsrc + (c >> 2) * 4096), "s"(gdst + (c >> 2) * 4096), "i"((c & 3) * 1024) : "memory");
          }
          SB();
          ++n;
        }
      }
    }
    MFMA_END();
    STAMP(8);
    BAR();
    LAS unsigned char* rw = lds + L_SCR + (w * 4 + g4) * RED_STR + l15 * 16;
#pragma unroll
    for (int c = 0; c < 8; ++c) *(LAS f32x4*)(rw + c * 256) = acc[c];
#define SG_OK() __all(sg[0].z == (unsigned)tag && sg[0].w == (unsigned)(tag >> 32) && sg[1].z == (unsigned)tag && sg[1].w == (unsigned)(tag >> 32) && \
                      sg[2].z == (unsigned)tag && sg[2].w == (unsigned)(tag >> 32) && sg[3].z == (unsigned)tag && sg[3].w == (unsigned)(tag >> 32))
    if (__builtin_expect(!SG_OK(), 0)) {
#pragma unroll 1
      for (unsigned s = 0; s < SPIN_CAP; ++s) {
        CFENCE();
#pragma unroll
        for (int i = 0; i < 4; ++i) sg[i] = LD128S(vl16, O_ST1 + w * 1024, i * 4096);
        if (SG_OK()) break;
      }
      __builtin_amdgcn_s_waitcnt(0x0F70);
    }
    BAR();
    const LAS unsigned char* rr_ = lds + L_SCR + (tid & 1) * 2 * RED_STR + (tid >> 1) * 16;
    f32x4 y4[2][2];
    v4u kr[2];
#pragma unroll
    for (int c = 0; c < 2; ++c) {
      kr[c] = *(const LAS v4u*)(lds + L_UR + c * UR_STR + (255 - tid) * 16);
#pragma unroll
      for (int hf = 0; hf < 2; ++hf)
        y4[c][hf] = *(const LAS f32x4*)(rr_ + (8 * c + hf) * RED_STR) + *(const LAS f32x4*)(rr_ + (8 * c + 4 + hf) * RED_STR);
    }
    const f32x2 D2 = {lo16(Dpair), hi16(Dpair)};
    unsigned xp[8], yp[8];
#pragma unroll
    for (int u = 0; u < 8; ++u) {
      const unsigned sv = sg[u >> 1][u & 1];
      const float mu = lo16(sv), ri = __builtin_amdgcn_rcpf(hi16(sv));
      const unsigned k0 = kr[0][(7 - u) >> 1], k1 = kr[1][(7 - u) >> 1], q0 = qg[0][u >> 1], q1 = qg[1][u >> 1];
      const f32x2 kv2 = (u & 1) ? f32x2{lo16(k0), lo16(k1)} : f32x2{hi16(k0), hi16(k1)};
      const f32x2 q2 = (u & 1) ? f32x2{hi16(q0), hi16(q1)} : f32x2{lo16(q0), lo16(q1)};
      const f32x2 y = __builtin_elementwise_fma(kv2, D2, f32x2{y4[0][u >> 2][u & 3], y4[1][u >> 2][u & 3]});
      yp[u] = pk(y.x, y.y);
      const f32x2 x = __builtin_elementwise_fma((q2 - mu) * ri, f32x2{lo16(yp[u]), hi16(yp[u])}, q2);
      xp[u] = pk(x.x, x.y);
    }
    const int vo = vox;
#pragma unroll
    for (int hf = 0; hf < 2; ++hf) ST128S((v4u{xp[4 * hf], xp[4 * hf + 1], xp[4 * hf + 2], xp[4 * hf + 3]}), vo, O_X1 + hf * 32 * XS + p * 128, 0);
#pragma unroll
    for (int hf = 0; hf < 2; ++hf) ST128S((v4u{yp[4 * hf], yp[4 * hf + 1], yp[4 * hf + 2], yp[4 * hf + 3]}), vo, O_Y + hf * 32 * XS + p * 128, 0);
    STAMP(9);
    bcv = (__uint_as_float(csp[0]) + __uint_as_float(csp[1])) + (__uint_as_float(csp[2]) + __uint_as_float(csp[3]));
    if (tid < 112) {
      *(LAS unsigned*)(lds + L_BC + tid * 4) = b1v << 16;
      *(LAS float*)(lds + L_BC + 512 + tid * 4) = bcv;
    }
    DRAIN();
    BAR();
    if (tid < 32) __hip_atomic_fetch_add((gu64*)(ws + O_C1 + (tid * 8 + g) * 128), 1ull, RLX_AGENT);
    if (w != 0) {
#pragma unroll 1
      for (int c = 4 * NG1 + w - 1; c < 111; c += 3)
        asm volatile("s_mov_b32 m0, %2\n\ts_nop 0\n\tglobal_load_lds_dwordx4 %0, %1 sc1"
                     :: "v"(vl16), "s"((u64)(uintptr_t)wsp + O_W1T + jc * 114688 + c * 1024), "s"((unsigned)(uintptr_t)lds + L_W1 + c * 1024) : "memory");
    }
    STAMP(10);
  }
  v4u w2r[14][4];
  const int sw2 = O_W2T + (cs * 56 + 14 * w) * 4096;
  v2u b2r[4];
#pragma unroll
  for (int nb = 0; nb < 4; ++nb) b2r[nb] = b2[16 * cs + 4 * nb + g4];
#pragma unroll
  for (int i = 0; i < 2; ++i) ST128S(ONES4, vl16, O_X3 + vbo * 32768 + (4 * cs + eo) * 1024, i * 2048);
  ST64S((v2u{~0u, ~0u}), lane < 16 ? vl16 : O_END, O_ST3 + (vbo * 2 + eo) * 1024 + (cs >> 1) * 256 + (cs & 1) * 8, 0);
  if (VAL2) {
#pragma unroll
    for (int i = 0; i < 4; ++i) FILL_H(O_H0, i);
  }
  wait_flags((gu64*)(ws + O_FB + ((112 + 2 * (14 * w + (lane < 14 ? lane : 0)) + (cs >> 2)) * 4 + (cs & 3)) * 64), tag);
  asm volatile("" :: "v"(b2r[0]), "v"(b2r[1]), "v"(b2r[2]), "v"(b2r[3]));
  DRAIN();
#pragma unroll
  for (int i = 0; i < W2A; ++i) w2r[i >> 2][i & 3] = LD128S(vl16, sw2, i * 1024);
  SB();
  if (w == 0) {
    if (SLP_S1) __builtin_amdgcn_s_sleep(SLP_S1);
    wait_flags((gu64*)(ws + O_C1 + ((b & 31) * 8 + (lane & 7)) * 128), (tag << 8) + 32);
  }
  BAR();
  if (b == 0 && tid == 0) __hip_atomic_store((gu64*)(ws + O_E), tag, RLX_AGENT);
  STAMP(11);

  unsigned xres[8], yres[8];
  const LAS unsigned char* wl0 = lds + vl16;
  const LAS unsigned char* wl1 = lds + vl16 + 57344;
  const LAS unsigned char* bcl = lds + L_BC + g4 * 16;
  f32x2 k1 = {-2.3022082f, -2.3022082f}, k3 = {-0.10294324f, -0.10294324f};
  asm volatile("" : "+v"(wl0), "+v"(wl1), "+v"(bcl), "+v"(k1), "+v"(k3));
#define W1F(f) (*(const LAS v4u*)((f) < 56 ? wl0 + (f) * 1024 : wl1 + ((f) - 56) * 1024))
#define BV(nb) (*(const LAS f32x4*)(bcl + 64 * (nb)))
#define CV(nb) (*(const LAS f32x4*)(bcl + 512 + 64 * (nb)))

  auto layer = [&](auto Lc) __attribute__((always_inline)) {
    constexpr int L = decltype(Lc)::value;
    const int OX = L ? O_X3 + vb * 32768 : O_X1 + vb * XS;
    const int OH = L ? O_H1 : O_H0;
    v4u F[16][2];
    f32x4 a1[4][2], aS[2], aQ[2];
    v4u st3[2];
    float rr[2], mr[2];
#pragma unroll 1
    for (unsigned att = 0; att < SPIN_CAP; ++att) {
      constexpr int WIN = 6;
      unsigned vm = 0;
      CFENCE();
      v4u raw[16][2];
#pragma unroll
      for (int ks = 0; ks < WIN; ++ks) { raw[ks][0] = LD128S(vl16, OX, ks * 2048); raw[ks][1] = LD128S(vl16, OX, ks * 2048 + 1024); }
      v4u ones = {0x3F803F80u, 0x3F803F80u, 0x3F803F80u, 0x3F803F80u};
      if (L == 0) asm volatile("" : "+v"(ones));
      v4u W[16][4];
#pragma unroll
      for (int nb = 0; nb < 4; ++nb) W[0][nb] = W1F(nb);
#define SHUF(ks, n) do { const int e_ = (n) & 1, i_ = ((n) >> 1) & 1; unsigned t_; \
        if ((n) == 0) asm volatile("s_mov_b32 vcc_lo, 0x00ff00ff\n\ts_mov_b32 vcc_hi, 0x00ff00ff" ::: "vcc"); \
        if ((n) == 4) asm volatile("s_not_b64 vcc, vcc" ::: "vcc", "scc"); \
        if ((n) < 4) { asm volatile("v_cndmask_b32_dpp %0, %1, %2, vcc row_ror:8 row_mask:0xf bank_mask:0xf" : "=v"(t_) \
                         : "v"(raw[ks][i_][2 + e_]), "v"(raw[ks][i_][e_]) : "vcc"); F[ks][e_][2 * i_] = t_; } \
        else { asm volatile("v_cndmask_b32_dpp %0, %1, %2, vcc row_ror:8 row_mask:0xf bank_mask:0xf" : "=v"(t_) \
                         : "v"(raw[ks][i_][e_]), "v"(raw[ks][i_][2 + e_]) : "vcc"); F[ks][e_][2 * i_ + 1] = t_; } } while (0)
      if (L == 0) {
#pragma unroll
        for (int n = 0; n < 8; ++n) SHUF(0, n);
      }
      MFMA_BEGIN();
      constexpr int NM = 12;
#pragma unroll
      for (int ks = 0; ks < 16; ++ks) {
        if (L == 1) { F[ks][0] = raw[ks][0]; F[ks][1] = raw[ks][1]; }
#pragma unroll
        for (int n = 0; n < NM; ++n) {
          const int e = n & 1, nb = n >> 1;
          if (L == 1 && n >= 8) continue;
          if (n < 8) { if (ks == 0) MF0(a1[nb][e], W[ks][nb], F[ks][e]); else MF(a1[nb][e], W[ks][nb], F[ks][e]); }
          else if (n < 10) { if (ks == 0) MF0(aS[e], ones, F[ks][e]); else MF(aS[e], ones, F[ks][e]); }
          else { if (ks == 0) MF0(aQ[e], F[ks][e], F[ks][e]); else MF(aQ[e], F[ks][e], F[ks][e]); }
          if (ks < 15 && n < 4) W[ks + 1][n] = W1F((ks + 1) * 7 + n);
          if (ks + WIN < 16 && (n == 4 || n == 5)) raw[ks + WIN][n - 4] = LD128S(vl16, OX, (ks + WIN) * 2048 + (n - 4) * 1024);
          if (L == 0 && (n == 10 || n == 11) && 2 * ks + n - 10 - 8 >= W2A) { const int x = 2 * (ks - 4) + n - 10; w2r[x >> 2][x & 3] = LD128S(vl16, sw2, x * 1024); }
          if (L == 1 && ks == 10 && (n == 4 || n == 5)) st3[n - 4] = LD128S(vl16, O_ST3 + vb * 2048, (n - 4) * 1024);
          if (L == 0 && ks < 15 && n >= 2 && n < 10) SHUF(ks + 1, n - 2);
          if (L == 1 && n == 6) vm = umax3(vm, raw[ks][0].w, raw[ks][1].w);
          SB();
        }
      }
      MFMA_END();
      if (L == 1) vm = umax3(umax3(vm, st3[0].x, st3[0].y), umax3(st3[0].z, st3[0].w, st3[1].x), umax3(st3[1].y, st3[1].z, st3[1].w));
      if (L == 0 || __builtin_expect(!__any(vm == ~0u), 1)) break;
    }
    STAMP(12 + 10 * L);
    f32x4 bq[2][2], cq[2][2];
    bq[0][0] = BV(0); cq[0][0] = CV(0); bq[0][1] = BV(1); cq[0][1] = CV(1);
    bq[1][0] = BV(2); cq[1][0] = CV(2); bq[1][1] = BV(3); cq[1][1] = CV(3);
#pragma unroll
    for (int e = 0; e < 2; ++e) {
      float sm, sq;
      if (L == 0) {
        const float d = (lane & 2) ? ((lane & 1) ? aQ[e][3] : aQ[e][2]) : ((lane & 1) ? aQ[e][1] : aQ[e][0]);
        sq = __uint_as_float(__builtin_amdgcn_ds_bpermute((l15 + 16 * (l15 >> 2)) * 4, __float_as_uint(d)));
        sm = aS[e][0];
      } else {
        sm = sum32x(sum16x(__uint_as_float(st3[e].x) + __uint_as_float(st3[e].z)));
        sq = sum32x(sum16x(__uint_as_float(st3[e].y) + __uint_as_float(st3[e].w)));
      }
      const float mu = sm * (1.0f / 512.0f);
      const float var = __builtin_fmaxf(__builtin_fmaf(-mu, mu, sq * (1.0f / 512.0f)), 0.f);
      const float r1 = __builtin_amdgcn_rsqf(var + EPS);
      const float r = r1 * __builtin_amdgcn_rsqf(__builtin_fmaf(var * r1, r1, EPS));
      rr[e] = r;
      mr[e] = -mu * r;
    }
    SB();
#pragma unroll
    for (int i = 0; i < 2; ++i)
#pragma unroll
      for (int e = 0; e < 2; ++e) {
        const f32x2 ac[4] = {lo2(a1[2 * i][e]), hi2(a1[2 * i][e]), lo2(a1[2 * i + 1][e]), hi2(a1[2 * i + 1][e])};
        const f32x2 bb[4] = {lo2(bq[i][0]), hi2(bq[i][0]), lo2(bq[i][1]), hi2(bq[i][1])};
        const f32x2 cc[4] = {lo2(cq[i][0]), hi2(cq[i][0]), lo2(cq[i][1]), hi2(cq[i][1])};
        unsigned o[4];
        gelu_pairs<4, false>(ac, bb, cc, rr[e], mr[e], k1, k3, o);
        ST128S((v4u{o[0], o[1], o[2], o[3]}), vl16, OH + (vb * 112 + hks + 2 * jq) * 1024, (e * 56 + i) * 1024);
        if (L == 0 && 2 * i + e < 3) {
#pragma unroll
          for (int t = 0; t < 4; ++t) { const int x = 24 + 4 * (2 * i + e) + t; w2r[x >> 2][x & 3] = LD128S(vl16, sw2, x * 1024); }
        }
        SB();
      }
    STAMP(13 + 10 * L);
    f32x4 a2[3][2];
    f32x4 bp[3], cpv[3];
    gu64* fh = (gu64*)(ws + O_FH + (((L * 2) * 64 + 8 * g + 2 * rq + ((lane >> 2) & 1)) * 16 + 4 * w + (lane & 3)) * 64);
    u64 fv;
    {
      v4u W[16][3];
#pragma unroll
      for (int ks = 0; ks < 2; ++ks)
#pragma unroll
        for (int nb = 0; nb < 3; ++nb) W[ks][nb] = W1F(ks * 7 + 4 + nb);
      MFMA_BEGIN();
#pragma unroll
      for (int ks = 0; ks < 16; ++ks) {
#pragma unroll
        for (int n = 0; n < 6; ++n) {
          const int e = n & 1, nb = n >> 1;
          if (ks == 0) MF0(a2[nb][e], W[ks][nb], F[ks][e]);
          else if (ks == 15 && nb == 2) MFA(a2[nb][e], wlast, F[ks][e]);
          else MF(a2[nb][e], W[ks][nb], F[ks][e]);
          if (ks < 14 && e == 0 && !(ks == 13 && nb == 2)) W[ks + 2][nb] = W1F((ks + 2) * 7 + 4 + nb);
          if (ks == 14 && e == 0) bp[nb] = BV(4 + nb);
          if (ks == 15 && e == 0) cpv[nb] = CV(4 + nb);
          if (L == 0 && (n == 1 || n == 3) && ks < 10) {
            const int i = 36 + 2 * ks + (n >> 1);
            w2r[i >> 2][i & 3] = LD128S(vl16, sw2, i * 1024);
          }
          if (L == 0 && ks >= 10 && ks < 14 && n != 2 && n != 5) {
            const int i = 4 * (ks & 1) + (n < 2 ? n : n - 1);
            const int so = vbo * XS + cs * 4096 + eo * 4, vo = g4 * 256 + (l15 & 7) * 16 + (l15 >> 3) * 8;
            if (ks < 12) xres[i] = LD32S(vo, O_X1 + so, (i >> 1) * 1024 + (i & 1) * 128);
            else yres[i] = LD32S(vo, O_Y + so, (i >> 1) * 1024 + (i & 1) * 128);
          }
          if (ks == DR1 && n == 5) {
            if (L == 0) asm volatile("s_waitcnt vmcnt(%0)" :: "i"(DR1 < 10 ? 2 * (DR1 + 1) : 20) : "memory"); else DRAIN();
            POST(O_FH + (((L * 2) * 64 + vb) * 16 + jc) * 64);
          }
          SB();
        }
      }
      MFMA_END();
    }
    STAMP(14 + 10 * L);
    int hb[4];
#pragma unroll
    for (int u = 0; u < 4; ++u) hb[u] = OH + ((16 * g + 4 * rq + ((w + u) & 3)) * 56 + 14 * w) * 1024;
    constexpr int WIN2 = 3;
    v4u hf[14][4];
#define LOAD_H(ks, u) do { if ((ks) < 12) hf[ks][u] = LD128S(vl16, hb[u], (ks) * 1024); \
        else { const v2u x0 = LD64S(vl8, hb[u], (ks) * 1024), x1 = LD64S(vl8, hb[u], (ks) * 1024 + 512); hf[ks][u] = v4u{x0.x, x0.y, x1.x, x1.y}; } } while (0)
#pragma unroll
    for (int e = 0; e < 2; ++e) {
      if (e == 0) { fv = __hip_atomic_load(fh, RLX_AGENT); SB(); }
      if (e == 1) {
        if (__builtin_expect(!__all(fv == tag), 0)) wait_flags(fh, tag);
        CFENCE();
#pragma unroll
        for (int ks = 0; ks < WIN2; ++ks)
#pragma unroll
          for (int u = 0; u < 4; ++u) LOAD_H(ks, u);
        SB();
      }
      const f32x2 ac[6] = {lo2(a2[0][e]), hi2(a2[0][e]), lo2(a2[1][e]), hi2(a2[1][e]), lo2(a2[2][e]), hi2(a2[2][e])};
      const f32x2 bb[6] = {lo2(bp[0]), hi2(bp[0]), lo2(bp[1]), hi2(bp[1]), lo2(bp[2]), hi2(bp[2])};
      const f32x2 cc[6] = {lo2(cpv[0]), hi2(cpv[0]), lo2(cpv[1]), hi2(cpv[1]), lo2(cpv[2]), hi2(cpv[2])};
      unsigned o[6];
      gelu_pairs<6, VAL2>(ac, bb, cc, rr[e], mr[e], k1, k3, o);
      ST128S((v4u{o[0], o[1], o[2], o[3]}), vl16, OH + (vb * 112 + hks + 8 + jq) * 1024, e * 56 * 1024);
      ST64S((v2u{o[4], o[5]}), vl8, OH + (vb * 112 + hks + 12 + (jq >> 1)) * 1024 + (jq & 1) * 512, e * 56 * 1024);
      SB();
    }
    if (VAL2) POST(O_FH + (((L * 2 + 1) * 64 + vb) * 16 + jc) * 64);
    STAMP(15 + 10 * L);
    f32x4 acc[4][4];
#pragma unroll 1
    for (unsigned att = 0; att < SPIN_CAP; ++att) {
      unsigned vm = 0;
      MFMA_BEGIN();
#pragma unroll
      for (int ks = 0; ks < 14; ++ks) {
        if (!VAL2 && ks == DR2) {
          asm volatile("s_waitcnt vmcnt(%0)" :: "i"(4 * DR2) : "memory");
          POST(O_FH + (((L * 2 + 1) * 64 + vb) * 16 + jc) * 64);
          SB();
        }
        if (ks == PL2) { fv = __hip_atomic_load(fh + 64 * 16 * 8, RLX_AGENT); SB(); }
        if (ks == CK2) {
          if (__builtin_expect(!__all(fv == tag), 0)) wait_flags(fh + 64 * 16 * 8, tag);
          CFENCE();
          MFMA_BEGIN();
        }
#pragma unroll
        for (int n = 0; n < 16; ++n) {
          const int u = n >> 2, nb = n & 3;
          if (ks == 0) MFA0(acc[u][nb], w2r[ks][nb], hf[ks][u]); else MFA(acc[u][nb], w2r[ks][nb], hf[ks][u]);
          if (nb == 0 && ks + WIN2 < 14 && (ks + WIN2 < 8 || ks >= CK2)) LOAD_H(ks == CK2 ? 8 : ks + WIN2, u);
          if (nb >= 1 && ks == CK2 && 8 + nb <= CK2 + WIN2) LOAD_H(8 + nb, u);
          if (VAL2 && ks >= 8 && nb == 1) vm = umax3(vm, hf[ks][u].y, hf[ks][u].w);
          SB();
        }
      }
      MFMA_END();
      if (!VAL2 || __builtin_expect(!__any(vm == ~0u), 1)) break;
      CFENCE();
#pragma unroll
      for (int ks = 0; ks < WIN2; ++ks)
#pragma unroll
        for (int u = 0; u < 4; ++u) LOAD_H(ks, u);
    }
    STAMP(17 + 10 * L);
#pragma unroll
    for (int u = 1; u < 4; ++u)
#pragma unroll
      for (int nb = 0; nb < 4; ++nb)
        *(LAS f32x4*)(lds + L_SCR + (((w + u) & 3) * 3 + (u - 1)) * 4096 + nb * 1024 + vl16) = acc[u][nb];
    f32x2 xo[8];
#pragma unroll
    for (int i = 0; i < 8; ++i) {
      const unsigned bb = b2r[i >> 1][i & 1];
      const f32x4 a = acc[0][i >> 1];
      xo[i] = ((i & 1) ? hi2(a) : lo2(a)) + f32x2{lo16(bb), hi16(bb)} + f32x2{lo16(xres[i]), hi16(xres[i])};
    }
    BAR();
#pragma unroll
    for (int nb = 0; nb < 4; ++nb) {
      const LAS unsigned char* rp = lds + L_SCR + w * 12288 + nb * 1024 + vl16;
      const f32x4 s = (*(const LAS f32x4*)rp + *(const LAS f32x4*)(rp + 4096)) + *(const LAS f32x4*)(rp + 8192);
      xo[2 * nb] += lo2(s);
      xo[2 * nb + 1] += hi2(s);
    }
    STAMP(18 + 10 * L);
    if (L == 0) {
      f32x2 s2 = {0.f, 0.f};
#pragma unroll
      for (int i = 0; i < 8; ++i) {
        const unsigned t = pk(xo[i].x, xo[i].y);
        xo[i] = f32x2{lo16(t), hi16(t)};
        s2 += xo[i];
      }
      const float mb = sum32x(sum16x(s2.x + s2.y)) * (1.0f / 64.0f);
      f32x2 q2 = {0.f, 0.f};
#pragma unroll
      for (int i = 0; i < 8; ++i) { const f32x2 t = xo[i] - mb; q2 = __builtin_elementwise_fma(t, t, q2); }
      const float m2 = sum32x(sum16x(q2.x + q2.y));
      const int so = O_ST2 + (vbo * 2 + eo) * 2048;
      if (lane < 16) ST128S((v4u{__float_as_uint(mb), __float_as_uint(m2), (unsigned)tag, (unsigned)(tag >> 32)}), vl16, so + cs * 256, 0);
      if (SLP_ST) __builtin_amdgcn_s_sleep(SLP_ST);
      v4u s0, s1;
#pragma unroll 1
      for (unsigned it = 0; it < SPIN_CAP; ++it) {
        CFENCE();
        s0 = LD128S(vl16, so, 0);
        s1 = LD128S(vl16, so, 1024);
        if (__all(s0.z == (unsigned)tag && s0.w == (unsigned)(tag >> 32) && s1.z == (unsigned)tag && s1.w == (unsigned)(tag >> 32))) break;
      }
      STAMP(19);
      const float ma = __uint_as_float(s0.x), mc = __uint_as_float(s1.x);
      const float mu = sum32x(sum16x(ma + mc)) * 0.125f;
      const float da = ma - mu, dc = mc - mu;
      const float tot = sum32x(sum16x(__uint_as_float(s0.y) + __uint_as_float(s1.y) + 64.0f * (da * da + dc * dc)));
      const float r = __builtin_amdgcn_rsqf(tot * (1.0f / 512.0f) + EPS);
#pragma unroll
      for (int i = 0; i < 8; ++i) {
        const f32x2 x = __builtin_elementwise_fma((xo[i] - mu) * r, f32x2{lo16(yres[i]), hi16(yres[i])}, xo[i]);
        xres[i] = san(pk(x.x, x.y));
      }
#pragma unroll
      for (int i = 0; i < 2; ++i)
        ST128S((v4u{xres[4 * i], xres[4 * i + 1], xres[4 * i + 2], xres[4 * i + 3]}), vl16, O_X3 + vbo * 32768 + (4 * cs + eo) * 1024, i * 2048);
      s2 = q2 = f32x2{0.f, 0.f};
#pragma unroll
      for (int i = 0; i < 8; ++i) {
        const f32x2 x = {lo16(xres[i]), hi16(xres[i])};
        s2 += x;
        q2 = __builtin_elementwise_fma(x, x, q2);
      }
      const float s3 = sum32x(sum16x(s2.x + s2.y)), q3 = sum32x(sum16x(q2.x + q2.y));
      if (lane < 16) ST64S((v2u{san(__float_as_uint(s3)), san(__float_as_uint(q3))}), vl16, O_ST3 + (vbo * 2 + eo) * 1024 + (cs >> 1) * 256 + (cs & 1) * 8, 0);
      POST(O_FX + ((vbo * 2 + eo) * 8 + cs) * 64);
      STAMP(20);
      if (VAL2) {
#pragma unroll
        for (int i = 0; i < 4; ++i) FILL_H(O_H1, i);
      }
      if (SLP_X3) __builtin_amdgcn_s_sleep(SLP_X3);
      wait_flags((gu64*)(ws + O_FX + (vb * 16 + l15) * 64), tag);
      STAMP(21);
    } else {
      const int t = 64 * (vbo & 31) + 8 * (l15 & 7) + 4 * (vbo >> 5) + 2 * (l15 >> 3) + eo;
      unsigned char* op = out + t * 1024 + cs * 128 + (g4 & 1) * 32 + (g4 >> 1) * 16;
#pragma unroll
      for (int i = 0; i < 2; ++i) {
        const auto r0 = __builtin_amdgcn_permlane16_swap(pk(xo[4 * i].x, xo[4 * i].y), pk(xo[4 * i + 2].x, xo[4 * i + 2].y), false, false);
        const auto r1 = __builtin_amdgcn_permlane16_swap(pk(xo[4 * i + 1].x, xo[4 * i + 1].y), pk(xo[4 * i + 3].x, xo[4 * i + 3].y), false, false);
        *(v4u*)(op + i * 64) = v4u{r0[0], r1[0], r0[1], r1[1]};
      }
      STAMP(29);
    }
  };
  layer(IC<0>{});
  layer(IC<1>{});
}

extern "C" void kernel_launch(void* const* d_in, const int* in_sizes, int n_in, void* d_out, int out_size, void* d_ws,
                              size_t ws_size, hipStream_t stream) {
  (void)in_sizes; (void)n_in; (void)out_size; (void)ws_size;
  (void)hipFuncSetAttribute((const void*)h3_mixer_kernel, hipFuncAttributeMaxDynamicSharedMemorySize, L_BYTES);
  h3_mixer_kernel<<<256, 256, L_BYTES, stream>>>(
      (const unsigned*)d_in[0], (const unsigned*)d_in[1], (const unsigned*)d_in[2], (const v4u*)d_in[3], (const v4u*)d_in[4],
      (const unsigned*)d_in[5], (const unsigned char*)d_in[7], (const u16*)d_in[8], (const unsigned char*)d_in[9], (const v2u*)d_in[10],
      (unsigned char*)d_out, (unsigned char*)d_ws);
}
```

```cpp
#include <hip/hip_runtime.h>
#include <stdint.h>

#define GAS __attribute__((address_space(1)))
#define LAS __attribute__((address_space(3)))
typedef unsigned v4u __attribute__((ext_vector_type(4)));
typedef unsigned v2u __attribute__((ext_vector_type(2)));
typedef float f32x4 __attribute__((ext_vector_type(4)));
typedef float f32x2 __attribute__((ext_vector_type(2)));
typedef short s16x4 __attribute__((ext_vector_type(4)));
typedef __bf16 bf16x2 __attribute__((ext_vector_type(2)));
typedef unsigned long long u64;
typedef GAS u64 gu64;
typedef unsigned short u16;

#define STAMP(i) do { } while (0)

namespace {
constexpr int NGC = 18, GS = 7, NGE = 22;
constexpr int MB = 30, MFL = 120, MS = 128;
constexpr int W2A0 = 8, W2A = 20, W2E = 12;
constexpr int DR1 = 5;
constexpr int WIN1 = 6, WIN2 = 3;
constexpr int RS0 = 11;
constexpr int SLP_ST = 11, SLP_X3 = 9;
constexpr int O_E = 0;
constexpr int O_FA = 4096;
constexpr int O_FB = O_FA + 65536;
constexpr int O_C1 = O_FB + 65536;
constexpr int O_FX = O_C1 + 32768;
constexpr int O_ST1 = O_FX + 65536;
constexpr int O_ST3 = O_ST1 + 16384;
constexpr int O_FH = O_ST3 + 131072;
constexpr int O_ST2 = O_FH + 131072;
constexpr int ROWB = 4224;
constexpr int O_KVR = O_ST2 + 262144;
constexpr int O_QT = O_KVR + 512 * ROWB;
constexpr int O_W2T = O_QT + 512 * ROWB;
constexpr int W2Q = 57344;
constexpr int O_X1 = O_W2T + 32 * W2Q;
constexpr int XS = 32768 + 128;
constexpr int X3S = 32768;
constexpr int O_Y = O_X1 + 64 * XS;
constexpr int O_X3 = O_Y + 64 * XS;
constexpr int O_H0 = O_X3 + 64 * X3S;
constexpr int HQ = 14 * 1024, HT = 4 * HQ;
constexpr int O_H1 = O_H0 + 128 * HT;
constexpr int O_END = O_H1 + 128 * HT;
constexpr int L_W1 = 0, L_SCR = 114688, L_BYTES = 163840;
constexpr int HS_STR = 4128, L_HS = L_SCR, L_UR = L_SCR + 33792, UR_STR = 4576, RED_STR = 2112;
constexpr unsigned SPIN_CAP = 1u << 16;
constexpr float EPS = 1e-5f;

#define RLX_AGENT __ATOMIC_RELAXED, __HIP_MEMORY_SCOPE_AGENT
#define DRAIN() asm volatile("s_waitcnt vmcnt(0)" ::: "memory")
#define CFENCE() asm volatile("" ::: "memory")
#define SB() __builtin_amdgcn_sched_barrier(0)
#define BAR() do { asm volatile("s_waitcnt lgkmcnt(0)" ::: "memory"); __builtin_amdgcn_s_barrier(); CFENCE(); } while (0)
#define LD32(off) __builtin_amdgcn_raw_buffer_load_b32(rs, (off), 0, 16)
#define LD128S(vo, so, c) __builtin_amdgcn_raw_buffer_load_b128(rs, (vo) + ((c) & 4095), (so) + ((c) & ~4095), 16)
#define LD64S(vo, so, c) __builtin_amdgcn_raw_buffer_load_b64(rs, (vo) + ((c) & 4095), (so) + ((c) & ~4095), 16)
#define ST128S(v, vo, so, c) do { const v4u d_ = (v); __builtin_amdgcn_raw_buffer_store_b128(d_, rs, (vo) + ((c) & 4095), (so) + ((c) & ~4095), 16); \
                                  asm volatile("s_nop 1" :: "v"(d_)); } while (0)
#define FILL128(vo, so, c) __builtin_amdgcn_raw_buffer_store_b128(ONES4, rs, (vo) + ((c) & 4095), (so) + ((c) & ~4095), 16)
#define ST64S(v, vo, so, c) __builtin_amdgcn_raw_buffer_store_b64((v), rs, (vo) + ((c) & 4095), (so) + ((c) & ~4095), 16)

template <int V> struct IC { static constexpr int value = V; };
template <int I, int N, class F> __device__ __forceinline__ void static_for(F&& f) {
  if constexpr (I < N) { f(IC<I>{}); static_for<I + 1, N>(f); }
}
__device__ __forceinline__ float lo16(unsigned x) { return __uint_as_float(x << 16); }
__device__ __forceinline__ float hi16(unsigned x) { return __uint_as_float(x & 0xffff0000u); }
__device__ __forceinline__ unsigned pk(float a, float b) {
  f32x2 v = {a, b};
  return __builtin_bit_cast(unsigned, __builtin_convertvector(v, bf16x2));
}
__device__ __forceinline__ float rbf(float a) { return lo16(pk(a, a)); }
#define MF(acc, a, b) asm volatile("v_mfma_f32_16x16x32_bf16 %0, %1, %2, %0" : "+v"(acc) : "v"(a), "v"(b))
#define MF0(acc, a, b) asm volatile("v_mfma_f32_16x16x32_bf16 %0, %1, %2, 0" : "=&v"(acc) : "v"(a), "v"(b))
#define MFA(acc, a, b) asm volatile("v_mfma_f32_16x16x32_bf16 %0, %1, %2, %0" : "+v"(acc) : "a"(a), "v"(b))
#define MFA0(acc, a, b) asm volatile("v_mfma_f32_16x16x32_bf16 %0, %1, %2, 0" : "=&v"(acc) : "a"(a), "v"(b))
#define MFMA_BEGIN() do { SB(); asm volatile("s_nop 1"); SB(); } while (0)
#define MFMA_END() do { SB(); asm volatile("s_nop 7"); SB(); } while (0)
__device__ __forceinline__ v2u tr_read(const LAS unsigned char* a) {
  return __builtin_bit_cast(v2u, __builtin_amdgcn_ds_read_tr16_b64_v4i16((LAS s16x4*)a));
}
template <int C> __device__ __forceinline__ float dppf(float v) {
  return __uint_as_float(__builtin_amdgcn_update_dpp(0u, __float_as_uint(v), C, 0xf, 0xf, true));
}
__device__ __forceinline__ float sum16x(float v) {
  auto r = __builtin_amdgcn_permlane16_swap(__float_as_uint(v), __float_as_uint(v), false, false);
  return __uint_as_float(r[0]) + __uint_as_float(r[1]);
}
__device__ __forceinline__ float sum32x(float v) {
  auto r = __builtin_amdgcn_permlane32_swap(__float_as_uint(v), __float_as_uint(v), false, false);
  return __uint_as_float(r[0]) + __uint_as_float(r[1]);
}
__device__ __forceinline__ float wave_sum(float v) {
  v += dppf<0xB1>(v);
  v += dppf<0x4E>(v);
  v += dppf<0x141>(v);
  v += dppf<0x140>(v);
  return sum32x(sum16x(v));
}
__device__ __forceinline__ void wait_flags(gu64* p, u64 want) {
#pragma unroll 1
  for (unsigned s = 0; s < SPIN_CAP; ++s) {
    const u64 x = __hip_atomic_load(p, RLX_AGENT);
    if (__all(x == want)) break;
  }
  __builtin_amdgcn_s_waitcnt(0x0F70);
  CFENCE();
}
__device__ __forceinline__ unsigned san(unsigned x) { return x < 0xFFFFFFFEu ? x : 0xFFFFFFFEu; }
__device__ __forceinline__ unsigned umax3(unsigned a, unsigned b, unsigned c) { const unsigned m = a > b ? a : b; return m > c ? m : c; }
__device__ __forceinline__ f32x2 lo2(f32x4 v) { return f32x2{v[0], v[1]}; }
__device__ __forceinline__ f32x2 hi2(f32x4 v) { return f32x2{v[2], v[3]}; }
template <int N, bool SAN, class H>
__device__ __forceinline__ void gelu_pairs(const f32x2 (&acc)[N], const f32x2 (&bb)[N], const f32x2 (&cc)[N], float r, float mr,
                                           f32x2 k1, f32x2 k3, unsigned (&o)[N], H&& hook) {
  const f32x2 r2 = {r, r}, m2 = {mr, mr};
  f32x2 u[N], z[N];
#pragma unroll
  for (int i = 0; i < N; ++i) u[i] = __builtin_elementwise_fma(m2, cc[i], bb[i]);
#pragma unroll
  for (int i = 0; i < N; ++i) u[i] = __builtin_elementwise_fma(acc[i], r2, u[i]);
#pragma unroll
  for (int i = 0; i < N; ++i) z[i] = __builtin_elementwise_fma(u[i] * u[i], k3, k1) * u[i];
  SB();
  hook(IC<0>{});
#pragma unroll
  for (int i = 0; i < N; ++i) z[i] = f32x2{__builtin_amdgcn_exp2f(z[i].x), __builtin_amdgcn_exp2f(z[i].y)};
  SB();
  hook(IC<1>{});
#pragma unroll
  for (int i = 0; i < N; ++i) z[i] += 1.0f;
  SB();
#pragma unroll
  for (int i = 0; i < N; ++i) z[i] = f32x2{__builtin_amdgcn_rcpf(z[i].x), __builtin_amdgcn_rcpf(z[i].y)};
  SB();
  hook(IC<2>{});
#pragma unroll
  for (int i = 0; i < N; ++i) { const f32x2 y = u[i] * z[i]; o[i] = SAN ? san(pk(y.x, y.y)) : pk(y.x, y.y); }
}

constexpr int conv_sg(int n) { int s = 0; while (n >= 8 - s) { n -= 8 - s; ++s; } return s; }
constexpr int conv_dl(int n) { int s = 0; while (n >= 8 - s) { n -= 8 - s; ++s; } return n; }
constexpr int ngl(int m) { return m < 1 ? 0 : ((m - 1) / GS + 1 < NGC ? (m - 1) / GS + 1 : NGC); }
constexpr int w2p(int ks) { return ks * (56 - W2A - W2E) / 16; }
static_assert(56 - W2A - W2E <= 32, "every fragment is requested");
#define POST(off) do { if (lane == 0) __hip_atomic_store((gu64*)(ws + (off)), tag, RLX_AGENT); } while (0)
#define LDV128(dst, so, c) asm volatile("buffer_load_dwordx4 %0, %1, %2, %3 offen offset:%4 sc1" : "=&v"(dst) \
                                        : "v"(vl16), "s"(rsd), "s"((so) + ((c) & ~4095)), "i"((c) & 4095) : "memory")

}

__global__ void __launch_bounds__(256, 1)
h3_mixer_kernel(const unsigned* __restrict__ q, const unsigned* __restrict__ k, const unsigned* __restrict__ v,
                const v4u* __restrict__ sk, const v4u* __restrict__ hk, const unsigned* __restrict__ Dv,
                const unsigned char* __restrict__ w1, const u16* __restrict__ b1, const unsigned char* __restrict__ w2,
                const v2u* __restrict__ b2, unsigned char* __restrict__ out, unsigned char* wsp) {
  extern __shared__ __attribute__((aligned(16))) unsigned char lds_raw[];
  LAS unsigned char* lds = (LAS unsigned char*)lds_raw;
  GAS unsigned char* ws = (GAS unsigned char*)wsp;
  const __amdgpu_buffer_rsrc_t rs = __builtin_amdgcn_make_buffer_rsrc(wsp, 0, O_END, 0x00020000);
  const v4u rsd = {(unsigned)(uintptr_t)wsp, (unsigned)((uintptr_t)wsp >> 32) & 0xffffu, (unsigned)O_END, 0x00020000u};
  const int b = blockIdx.x, tid = threadIdx.x, lane = tid & 63, w = __builtin_amdgcn_readfirstlane(tid >> 6);
  const int g = b & 7, j = b >> 3;
  const int l15 = lane & 15, g4 = lane >> 4;
  const int rh = j >> 4, jc = j & 15, rq = j >> 3, cs = j & 7;
  const int p = g * 32 + j;
  const int vl16 = lane * 16, vl8 = lane * 8;
  STAMP(0);

  u64 pc0;
  unsigned clen;
  asm volatile("s_getpc_b64 %0\n.Lh3_begin:\n\ts_mov_b32 %1, .Lh3_end-.Lh3_begin" : "=s"(pc0), "=s"(clen));
  const unsigned pfo = (((j & 7) * 4 + w) * 8 + (lane & 7)) * 64 + 512;
#define CODE_PF(dst) do { u64 pc_; asm volatile("s_getpc_b64 %0" : "=s"(pc_)); unsigned o_ = (unsigned)(pc_ - pc0) + pfo; \
                          o_ = o_ < clen ? o_ : clen; dst = *(const GAS unsigned*)((pc0 + o_) & ~3ull); } while (0)
  unsigned pre;
  CODE_PF(pre);
  SB();
  const int cp = tid & 31, tq = tid >> 5;
  unsigned kk[11], vv[8], qq[8];
  const int r0 = 64 * j + 8 * tq, col = 32 * g + cp;
#pragma unroll
  for (int i = 0; i < 11; ++i) {
    const int r = r0 - 3 + i;
    kk[i] = k[(r < 0 ? 0 : r) * 256 + col];
  }
#pragma unroll
  for (int i = 0; i < 8; ++i) vv[i] = v[(r0 + i) * 256 + col];
  const v4u tap = sk[col];
  const u64 e0 = __hip_atomic_load((gu64*)(ws + O_E), RLX_AGENT);
#pragma unroll
  for (int i = 0; i < 8; ++i) qq[i] = q[(r0 + i) * 256 + col];
  {
    const unsigned co = (b * 4 + w) * 64;
    const u64 a = (lane & 3) == 0 ? (u64)(uintptr_t)wsp + O_FA + (b * 4 + w) * 64
                : (lane & 3) == 1 ? (u64)(uintptr_t)wsp + O_ST1 + (w * 256 + b) * 16
                : ((pc0 + (co < clen ? co : clen)) & ~3ull);
    pre += __hip_atomic_load((GAS unsigned*)a, RLX_AGENT);
  }
  v4u qrow[2];
#pragma unroll
  for (int i = 0; i < 2; ++i) qrow[i] = ((const v4u*)q)[(8 * b + 2 * w + i) * 64 + lane];
  v4u hg[2], hp[2], wraw[4];
#pragma unroll
  for (int c = 0; c < 2; ++c) {
    hg[c] = hk[(2 * p + c) * 256 + tid];
    hp[c] = hk[(2 * p + c) * 256 + (tid >= 64 ? (tid | 63) - 64 : 0)];
  }
  pre += *(const unsigned*)((tid < 128 ? w1 : w2) + b * 7168 + ((tid & 127) < 112 ? (tid & 127) : 0) * 64);
  SB();
  const int vb = 8 * g + 4 * rh + w;
  const int vbo = 8 * g + 2 * rq + (w >> 1), eo = w & 1;
  const int jq = jc & 3, hbo = vb * 2 * HT + (jc >> 2) * HQ;
  const int w2u = b < 112 ? b : 0, ksg = w2u >> 1;
  const unsigned char* wsrc;
  int wdB;
  {
    const int wq = ksg / 14, kl = ksg - 14 * wq;
    const int rowA = kl < 8 ? 112 * (4 * wq + (kl >> 1)) + 32 * (kl & 1)
                            : (kl < 12 ? 112 * (4 * wq + kl - 8) + 64 : 112 * (4 * wq + 2 * (kl - 12)) + 96);
    wdB = kl < 12 ? 0 : 96 * 1024;
    wsrc = w2 + (rowA + (lane >> 3)) * 1024 + (w2u & 1) * 512 + w * 128 + (lane & 7) * 16;
  }
  unsigned gvo[7];
#pragma unroll
  for (int i = 0; i < 7; ++i) { const unsigned gi = 64 * i + lane, row = gi / 14; gvo[i] = row * 3584 + (gi - 14 * row) * 16; }
  int pto;
  {
    const int to = vbo * 2 + eo;
    pto = l15 < 4 ? O_FH + (((l15 >> 1) * 64 + vb) * 16 + jc) * 64
        : l15 < 5 ? O_FX + (to * 8 + cs) * 64
        : l15 < 7 ? O_ST2 + to * 2048 + cs * 256 + (l15 - 5) * 128
        : l15 < 8 ? O_FB + (b * 4 + w) * 64
        : l15 < 10 ? O_ST3 + to * 1024 + (cs >> 1) * 256 + (l15 - 8) * 128
        : O_C1 + b * 128;
  }
  if (r0 == 0) kk[0] = kk[1] = kk[2] = 0;
  unsigned b1v[2], pre2;
  v2u b2r[4];
  const unsigned Dpair = Dv[p];
  const int vot = (tid >> 3) * ROWB + (tid & 7) * 16;
  LAS unsigned char* const stw = lds + (2 * cp) * 144;
  const LAS unsigned char* const str = lds + (tid >> 3) * 144 + (tid & 7) * 16;
  const u64 tag = ((u64)__builtin_amdgcn_readfirstlane((unsigned)(e0 >> 32)) << 32 | __builtin_amdgcn_readfirstlane((unsigned)e0)) +
                  0x9E3779B97F4A7C15ull;
  {
    const f32x2 t0 = {lo16(tap.x), lo16(tap.z)}, t1 = {hi16(tap.x), hi16(tap.z)}, t2 = {lo16(tap.y), lo16(tap.w)},
                t3 = {hi16(tap.y), hi16(tap.w)};
    f32x2 kf[11], r[8];
#pragma unroll
    for (int i = 0; i < 11; ++i) kf[i] = f32x2{lo16(kk[i]), hi16(kk[i])};
#pragma unroll
    for (int u = 0; u < 8; ++u)
      r[u] = (kf[u] * t3 + kf[u + 1] * t2 + kf[u + 2] * t1 + kf[u + 3] * t0) * f32x2{lo16(vv[u]), hi16(vv[u])};
    STAMP(1);
    *(LAS v4u*)(stw + (7 - tq) * 16) = v4u{pk(r[7].x, r[6].x), pk(r[5].x, r[4].x), pk(r[3].x, r[2].x), pk(r[1].x, r[0].x)};
    *(LAS v4u*)(stw + 144 + (7 - tq) * 16) = v4u{pk(r[7].y, r[6].y), pk(r[5].y, r[4].y), pk(r[3].y, r[2].y), pk(r[1].y, r[0].y)};
    *(LAS v4u*)(stw + 9216 + tq * 16) =
        v4u{__builtin_amdgcn_perm(qq[1], qq[0], 0x05040100u), __builtin_amdgcn_perm(qq[3], qq[2], 0x05040100u),
            __builtin_amdgcn_perm(qq[5], qq[4], 0x05040100u), __builtin_amdgcn_perm(qq[7], qq[6], 0x05040100u)};
    *(LAS v4u*)(stw + 9216 + 144 + tq * 16) =
        v4u{__builtin_amdgcn_perm(qq[1], qq[0], 0x07060302u), __builtin_amdgcn_perm(qq[3], qq[2], 0x07060302u),
            __builtin_amdgcn_perm(qq[5], qq[4], 0x07060302u), __builtin_amdgcn_perm(qq[7], qq[6], 0x07060302u)};
    BAR();
    if (j == 0 && tid < 32) __hip_atomic_store((gu64*)(ws + O_C1 + (tid * 8 + g) * 128), tag << 8, RLX_AGENT);
    const v4u ta0 = *(const LAS v4u*)str, ta1 = *(const LAS v4u*)(str + 32 * 144);
    const v4u tc0 = *(const LAS v4u*)(str + 9216), tc1 = *(const LAS v4u*)(str + 9216 + 32 * 144);
    ST128S(ta0, vot, O_KVR + 64 * g * ROWB + (248 - 8 * j) * 16, 0);
    ST128S(ta1, vot, O_KVR + (64 * g + 32) * ROWB + (248 - 8 * j) * 16, 0);
    ST128S(tc0, vot, O_QT + 64 * g * ROWB + 8 * j * 16, 0);
    ST128S(tc1, vot, O_QT + (64 * g + 32) * ROWB + 8 * j * 16, 0);
    SB();
    STAMP(2);
  }
  v4u ONES4 = {~0u, ~0u, ~0u, ~0u};
#define FILL_H(OH, i) do { const int e_ = (i) & 1; \
    if ((i) < 2) FILL128(vl16, (OH) + hbo + (8 + jq) * 1024, e_ * HT); \
    else ST64S((v2u{~0u, ~0u}), vl8, (OH) + hbo + (12 + (jq >> 1)) * 1024 + (jq & 1) * 512, e_ * HT); } while (0)
#define GLDS(c) asm volatile("s_mov_b32 m0, %2\n\ts_nop 0\n\tglobal_load_lds_dwordx4 %0, %1" \
                  :: "v"(gvo[(c) % 7]), "s"(gsrc - ((c) / 7) * 4 * 114688), "s"(gdst - ((c) / 7) * 4 * 7168 + ((c) % 7) * 1024) : "memory")
  const u64 gsrc = (u64)(uintptr_t)w1 + jc * 224 + (12 + w) * 114688;
  const unsigned gdst = (unsigned)(uintptr_t)lds + L_W1 + (12 + w) * 7168;
  {
#pragma unroll
    for (int c = 0; c < 2; ++c) {
      const v4u d = hg[c];
      v4u a;
#pragma unroll
      for (int i = 0; i < 4; ++i) a[i] = __builtin_amdgcn_update_dpp(w ? hp[c][i] : 0u, d[i], 0x138, 0xf, 0xf, false);
      const unsigned s0 = __builtin_amdgcn_alignbyte(a.y, a.x, 2), s1 = __builtin_amdgcn_alignbyte(a.z, a.y, 2),
                     s2 = __builtin_amdgcn_alignbyte(a.w, a.z, 2), s3 = __builtin_amdgcn_alignbyte(d.x, a.w, 2),
                     s4 = __builtin_amdgcn_alignbyte(d.y, d.x, 2);
      LAS unsigned char* o = lds + L_HS + c * 4 * HS_STR + (tid + 1) * 16;
      *(LAS v4u*)(o) = v4u{s0, s1, s2, s3};
      *(LAS v4u*)(o + HS_STR) = v4u{a.y, a.z, a.w, d.x};
      *(LAS v4u*)(o + 2 * HS_STR) = v4u{s1, s2, s3, s4};
      *(LAS v4u*)(o + 3 * HS_STR) = v4u{a.z, a.w, d.x, d.y};
      if (tid == 255) {
        const unsigned u1 = __builtin_amdgcn_alignbyte(d.z, d.y, 2), u2 = __builtin_amdgcn_alignbyte(d.w, d.z, 2), u3 = d.w >> 16;
        *(LAS v4u*)(o + 16) = v4u{s4, u1, u2, u3};
        *(LAS v4u*)(o + 16 + HS_STR) = v4u{d.y, d.z, d.w, 0};
        *(LAS v4u*)(o + 16 + 2 * HS_STR) = v4u{u1, u2, u3, 0};
        *(LAS v4u*)(o + 16 + 3 * HS_STR) = v4u{d.z, d.w, 0, 0};
      }
    }
    if (tid < 68) {
      const int i = tid - 8, c = i >= 30;
      *(LAS v4u*)(lds + (tid < 8 ? L_HS + tid * HS_STR : L_UR + c * UR_STR + 4096 + (i - 30 * c) * 16)) = v4u{0, 0, 0, 0};
    }
    SB();
    asm volatile("s_waitcnt vmcnt(0)" :: "v"(pre) : "memory");
    POST(O_FA + (b * 4 + w) * 64);
#pragma unroll
    for (int i = 0; i < 4; ++i) wraw[i] = *(const v4u*)(wsrc + i * 8192 + (i >= 2 ? wdB : 0));
#pragma unroll
    for (int nb = 0; nb < 4; ++nb) b2r[nb] = b2[16 * cs + 4 * nb + g4];
    b1v[0] = b1[112 * jc + lane];
    b1v[1] = b1[112 * jc + (lane < 48 ? 64 + lane : 0)];
    pre2 = LD32(pto);
    SB();
    STAMP(3);
  }
  BAR();
  const int ch = w >> 1, rho0 = 4 * (w & 1);
  const LAS unsigned char* pa = lds + L_HS + (ch * 4 + (lane & 3)) * HS_STR + 8 * ((l15 >> 2) + 2 * g4) + 64 * rho0;
  const LAS unsigned char* pb = lds + L_UR + ch * UR_STR + 2 * (240 - 16 * l15 + 8 * g4) + 64 * rho0;
  v4u A[2][8], B[2][8];
#pragma unroll
  for (int s = 0; s < 8; ++s) {
    const v2u a0 = *(const LAS v2u*)(pa + 512 * s), a1 = *(const LAS v2u*)(pa + 512 * s + 8);
    A[0][s] = v4u{a0.x, a0.y, a1.x, a1.y};
  }
  SB();
  {
    float ms[2][2];
#pragma unroll
    for (int i = 0; i < 2; ++i) {
      f32x2 x[4];
#pragma unroll
      for (int d = 0; d < 4; ++d) x[d] = f32x2{lo16(qrow[i][d]), hi16(qrow[i][d])};
      const f32x2 s = (x[0] + x[1]) + (x[2] + x[3]);
      const float m = wave_sum(s.x + s.y) * (1.0f / 512.0f);
      f32x2 sq = {0.f, 0.f};
#pragma unroll
      for (int d = 0; d < 4; ++d) { const f32x2 t = x[d] - m; sq = __builtin_elementwise_fma(t, t, sq); }
      const float var = wave_sum(sq.x + sq.y) * (1.0f / 512.0f);
      ms[i][0] = m;
      ms[i][1] = __builtin_amdgcn_sqrtf(rbf(rbf(var) + EPS));
    }
    unsigned msp0 = pk(ms[0][0], ms[0][1]), msp1 = pk(ms[1][0], ms[1][1]);
    asm volatile("" : "+v"(msp0), "+v"(msp1));
    ST128S((v4u{msp0, msp1, (unsigned)tag, (unsigned)(tag >> 32)}), lane ? O_END : 0, O_ST1 + (w * 256 + b) * 16, 0);
  }
  SB();
  CODE_PF(pre);
  v4u wf[4];
  {
    LAS unsigned char* wst = lds + w * 4096 + (lane >> 3) * 128 + ((lane & 7) ^ ((lane >> 3) & 6)) * 16;
    LAS unsigned char* wtr = lds + w * 4096 + (4 * g4 + ((lane >> 2) & 3)) * 128 + (lane & 3) * 8;
    const int wsw = (2 * g4 + ((lane >> 3) & 1)) & 3;
#pragma unroll
    for (int i = 0; i < 4; ++i) *(LAS v4u*)(wst + i * 1024) = wraw[i];
#pragma unroll
    for (int i = 0; i < 4; ++i) {
      const v2u x0 = tr_read(wtr + (i ^ wsw) * 32), x1 = tr_read(wtr + (i ^ wsw) * 32 + 2048);
      wf[i] = v4u{x0.x, x0.y, x1.x, x1.y};
    }
    asm volatile("" :: "v"(wf[0]), "v"(wf[1]), "v"(wf[2]), "v"(wf[3]));
  }
  SB();
  STAMP(4);
  wait_flags((gu64*)(ws + O_FA + (lane < 32 ? (lane * 8 + g) * 4 + ((j >> 2) & 3) : g * 4) * 64), tag);
  asm volatile("" :: "v"(pre), "v"(pre2), "v"(b1v[0]), "v"(b1v[1]), "v"(b2r[0]), "v"(b2r[1]), "v"(b2r[2]), "v"(b2r[3]));
  STAMP(5);
  v4u w2r[14][4];
  const int sw2 = O_W2T + (cs * 4 + w) * W2Q;
  gu64* fb2 = (gu64*)(ws + O_FB + ((2 * (14 * w + (lane < 14 ? lane : 0)) + (cs >> 2)) * 4 + (cs & 3)) * 64);
  v4u qg[2], sg[4];
  {
    v4u u[2];
#pragma unroll
    for (int c = 0; c < 2; ++c) LDV128(u[c], O_KVR + (2 * p + c) * ROWB + w * 1024, 0);
#pragma unroll
    for (int c = 0; c < 2; ++c) LDV128(qg[c], O_QT + (2 * p + c) * ROWB + w * 1024, 0);
    const int w2so = b < 112 ? O_W2T + ((4 * (b & 1) + w) * 4 + ksg / 14) * W2Q + (ksg % 14) * 4096 : O_END;
#pragma unroll
    for (int i = 0; i < 4; ++i) ST128S(wf[i], vl16, w2so, i * 1024);
    SB();
    asm volatile("s_waitcnt vmcnt(6)" : "+v"(u[0]), "+v"(u[1]) :: "memory");
#pragma unroll
    for (int c = 0; c < 2; ++c) *(LAS v4u*)(lds + L_UR + c * UR_STR + w * 1024 + vl16) = u[c];
  }
  BAR();
  STAMP(7);
  const int vox = (tid >> 3) * XS + (tid & 7) * 16;
  {
    f32x4 acc[8];
    v4u kr[2];
    float q2[8][2], kd[8][2];
#pragma unroll
    for (int s = 0; s < 8; ++s) { B[0][s] = *(const LAS v4u*)(pb + 512 * (7 - s)); SB(); }
    u64 fbv;
    static_assert(MB < MFL && MFL < MS && ngl(MS - 1) == NGC, "the counted waits assume this order");
    MFMA_BEGIN();
    static_for<0, 144>([&](auto mc) __attribute__((always_inline)) {
      constexpr int m = decltype(mc)::value, r = m / 36, n = m % 36, sgm = conv_sg(n), dl = conv_dl(n);
      if constexpr (r == 0 && sgm == 0) MF0(acc[dl], A[0][0], B[0][dl]);
      else MF(acc[sgm + dl], A[r & 1][sgm], B[r & 1][dl]);
      if constexpr (r < 3 && (n & 1) == 0 && (n >> 1) < 16) {
        constexpr int s = n >> 2;
        if constexpr ((n & 2) == 0) {
          B[(r + 1) & 1][s] = *(const LAS v4u*)(pb + 64 * (r + 1) + 512 * (7 - s));
        } else {
          const v2u a0 = *(const LAS v2u*)(pa + 64 * (r + 1) + 512 * s), a1 = *(const LAS v2u*)(pa + 64 * (r + 1) + 512 * s + 8);
          A[(r + 1) & 1][s] = v4u{a0.x, a0.y, a1.x, a1.y};
        }
      }
      if constexpr (m % GS == 1 && m / GS < NGC) GLDS(m / GS);
      if constexpr (m == MB) {
        asm volatile("s_waitcnt vmcnt(%2)" : "+v"(qg[0]), "+v"(qg[1]) : "i"(ngl(MB)) : "memory");
        POST(O_FB + (b * 4 + w) * 64);
      }
      if constexpr (m == MFL) asm volatile("global_load_dwordx2 %0, %1, off sc1" : "=&v"(fbv) : "v"(fb2 + (m - MFL)) : "memory");
      if constexpr (m == 104 || m == 105) kr[m - 104] = *(const LAS v4u*)(lds + L_UR + (m - 104) * UR_STR + (255 - tid) * 16);
      if constexpr (m == 110) BAR();
      if constexpr (m > 110 && m < 143) {
        constexpr int x = m - 111, u = x >> 2, c = x & 1;
        if constexpr (x & 2) { const unsigned q0 = qg[c][u >> 1]; q2[u][c] = (u & 1) ? hi16(q0) : lo16(q0); asm volatile("" : "+v"(q2[u][c])); }
        else { const unsigned k0 = kr[c][(7 - u) >> 1]; kd[u][c] = ((u & 1) ? lo16(k0) : hi16(k0)) * (c ? hi16(Dpair) : lo16(Dpair)); asm volatile("" : "+v"(kd[u][c])); }
      }
      if constexpr (m >= MS && m < MS + 4) LDV128(sg[m - MS], O_ST1 + w * 1024, (m - MS) * 4096);
      SB();
    });
    MFMA_END();
    STAMP(8);
    LAS unsigned char* rw = lds + L_SCR + (w * 4 + g4) * RED_STR + l15 * 16;
#pragma unroll
    for (int c = 0; c < 8; ++c) *(LAS f32x4*)(rw + c * 256) = acc[c];
    SB();
    asm volatile("s_waitcnt vmcnt(0)" : "+v"(sg[0]), "+v"(sg[1]), "+v"(sg[2]), "+v"(sg[3]), "+v"(fbv) :: "memory");
#define SG_BAD() __any((((sg[0].z ^ (unsigned)tag) | (sg[0].w ^ (unsigned)(tag >> 32))) | ((sg[1].z ^ (unsigned)tag) | (sg[1].w ^ (unsigned)(tag >> 32))) | \
                        ((sg[2].z ^ (unsigned)tag) | (sg[2].w ^ (unsigned)(tag >> 32))) | ((sg[3].z ^ (unsigned)tag) | (sg[3].w ^ (unsigned)(tag >> 32)))) != 0)
    if (__builtin_expect(SG_BAD(), 0)) {
#pragma unroll 1
      for (unsigned s = 0; s < SPIN_CAP; ++s) {
        CFENCE();
#pragma unroll
        for (int i = 0; i < 4; ++i) sg[i] = LD128S(vl16, O_ST1 + w * 1024, i * 4096);
        if (!SG_BAD()) break;
      }
      __builtin_amdgcn_s_waitcnt(0x0F70);
    }
#pragma unroll
    for (int c = NGC; c < NGE; ++c) GLDS(c);
    f32x2 qn[8];
#pragma unroll
    for (int u = 0; u < 8; ++u) {
      const unsigned sv = sg[u >> 1][u & 1];
      qn[u] = (f32x2{q2[u][0], q2[u][1]} - lo16(sv)) * __builtin_amdgcn_rcpf(hi16(sv));
      asm volatile("" : "+v"(qn[u]));
    }
    const LAS unsigned char* rr_ = lds + L_SCR + (tid & 1) * 2 * RED_STR + (tid >> 1) * 16;
    SB();
    BAR();
    f32x4 y4[2][2][2];
#pragma unroll
    for (int hf = 0; hf < 2; ++hf)
#pragma unroll
      for (int c = 0; c < 2; ++c) {
        y4[hf][c][0] = *(const LAS f32x4*)(rr_ + (8 * c + hf) * RED_STR);
        y4[hf][c][1] = *(const LAS f32x4*)(rr_ + (8 * c + 4 + hf) * RED_STR);
      }
    SB();
    v4u xp[2], yp[2];
#pragma unroll
    for (int hf = 0; hf < 2; ++hf) {
#pragma unroll
      for (int i = 0; i < 4; ++i) {
        const int u = 4 * hf + i;
        const float y0 = (y4[hf][0][0][i] + kd[u][0]) + y4[hf][0][1][i], y1 = (y4[hf][1][0][i] + kd[u][1]) + y4[hf][1][1][i];
        yp[hf][i] = pk(y0, y1);
        const f32x2 x = __builtin_elementwise_fma(qn[u], f32x2{lo16(yp[hf][i]), hi16(yp[hf][i])}, f32x2{q2[u][0], q2[u][1]});
        xp[hf][i] = pk(x.x, x.y);
      }
      __builtin_amdgcn_raw_buffer_store_b128(xp[hf], rs, vox, O_X1 + hf * 32 * XS + p * 128, 16);
      __builtin_amdgcn_raw_buffer_store_b128(yp[hf], rs, vox, O_Y + hf * 32 * XS + p * 128, 16);
      SB();
    }
    asm volatile("s_nop 0" :: "v"(xp[0]), "v"(yp[0]), "v"(xp[1]), "v"(yp[1]));
    STAMP(9);
#pragma unroll
    for (int c = NGE; c < 28; ++c) GLDS(c);
    asm volatile("s_waitcnt vmcnt(%0)" :: "i"(28 - NGE) : "memory");
    BAR();
    if (tid < 32) __hip_atomic_fetch_add((gu64*)(ws + O_C1 + (tid * 8 + g) * 128), 1ull, RLX_AGENT);
    if (__builtin_expect(!__all(fbv == tag), 0)) wait_flags(fb2, tag);
    CFENCE();
#pragma unroll
    for (int i = 0; i < W2A0; ++i) w2r[i >> 2][i & 3] = LD128S(vl16, sw2, i * 1024);
    SB();
    STAMP(10);
  }
  const LAS unsigned char* wl0 = lds + (4 * g4 + ((lane >> 2) & 3)) * 224 + (lane & 3) * 8;
  const LAS unsigned char* wl1 = wl0 + 8 * 7168;
  asm volatile("" : "+v"(wl0), "+v"(wl1));
  auto w1h = [&](int ks, int nb, int h) __attribute__((always_inline)) {
    const LAS unsigned char* a = (ks < 8 ? wl0 + ks * 7168 : wl1 + (ks - 8) * 7168) + nb * 32 + h * 3584;
    return tr_read(a);
  };
  auto w1f = [&](int ks, int nb) __attribute__((always_inline)) {
    const LAS unsigned char* a = (ks < 8 ? wl0 + ks * 7168 : wl1 + (ks - 8) * 7168) + nb * 32;
    const v2u x0 = tr_read(a), x1 = tr_read(a + 3584);
    return v4u{x0.x, x0.y, x1.x, x1.y};
  };
  {
    asm volatile("s_waitcnt vmcnt(%0)" :: "i"(W2A0) : "memory");
    v4u ones = {0x3F803F80u, 0x3F803F80u, 0x3F803F80u, 0x3F803F80u};
    asm volatile("" : "+v"(ones));
    const LAS unsigned char* a = wl0 + w * 7168;
    f32x4 ca[7];
#pragma unroll
    for (int i = 0; i < 4; ++i) {
      v4u f[7];
#pragma unroll
      for (int nb = 0; nb < 7; ++nb) {
        const v2u x0 = tr_read(a + i * 28672 + nb * 32), x1 = tr_read(a + i * 28672 + nb * 32 + 3584);
        f[nb] = v4u{x0.x, x0.y, x1.x, x1.y};
      }
      SB();
#pragma unroll
      for (int x = W2A0 + i * (W2A - W2A0) / 4; x < W2A0 + (i + 1) * (W2A - W2A0) / 4; ++x) w2r[x >> 2][x & 3] = LD128S(vl16, sw2, x * 1024);
      MFMA_BEGIN();
#pragma unroll
      for (int nb = 0; nb < 7; ++nb) { if (i == 0) MF0(ca[nb], f[nb], ones); else MF(ca[nb], f[nb], ones); }
    }
    MFMA_END();
    if (l15 == 0) {
#pragma unroll
      for (int nb = 0; nb < 7; ++nb) *(LAS f32x4*)(lds + L_SCR + w * 12288 + 2048 + nb * 64 + g4 * 16) = ca[nb];
    }
  }
  CODE_PF(pre2);
  BAR();
  float csv[2];
#pragma unroll
  for (int i = 0; i < 2; ++i) {
    const LAS float* pp = (const LAS float*)(lds + L_SCR + 2048) + 64 * i + lane;
    csv[i] = (pp[0] + pp[3072]) + (pp[6144] + pp[9216]);
  }
  asm volatile("" : "+v"(csv[0]), "+v"(csv[1]));
  STAMP(16);
  if (w == 0) wait_flags((gu64*)(ws + O_C1 + ((b & 31) * 8 + (lane & 7)) * 128), (tag << 8) + 32);
  BAR();
  if (b == 0 && tid == 0) __hip_atomic_store((gu64*)(ws + O_E), tag, RLX_AGENT);
  STAMP(11);

  asm volatile("" : "+v"(ONES4));
  unsigned xres[8], yres[8];
  LAS unsigned char* bcw = lds + L_SCR + ((w + 1) & 3) * 12288 + lane * 4;
  const LAS unsigned char* bcl = lds + L_SCR + ((w + 1) & 3) * 12288 + g4 * 16;
  f32x2 k1 = {-2.3022082f, -2.3022082f}, k3 = {-0.10294324f, -0.10294324f};
  asm volatile("" : "+v"(bcl), "+v"(k1), "+v"(k3));
#define W1F(f) w1f((f) / 7, (f) % 7)
#define BV(nb) (*(const LAS f32x4*)(bcl + 64 * (nb)))
#define CV(nb) (*(const LAS f32x4*)(bcl + 512 + 64 * (nb)))

  auto layer = [&](auto Lc) __attribute__((always_inline)) {
    constexpr int L = decltype(Lc)::value;
    const int OX = L ? O_X3 + vb * X3S : O_X1 + vb * XS;
    const int OH = L ? O_H1 : O_H0;
    v4u F[16][2];
    f32x4 a1[4][2], aS[2], aQ[2], bq[2][2], cq[2][2];
    v4u st3[2];
    float rr[2], mr[2];
#pragma unroll 1
    for (unsigned att = 0; att < SPIN_CAP; ++att) {
      unsigned vm = 0;
      CFENCE();
      v4u raw[16][2];
#pragma unroll
      for (int ks = 0; ks < WIN1; ++ks) { raw[ks][0] = LD128S(vl16, OX, ks * 2048); raw[ks][1] = LD128S(vl16, OX, ks * 2048 + 1024); SB(); }
      if (L == 0) asm volatile("" :: "v"(pre2));
      v4u ones = {0x3F803F80u, 0x3F803F80u, 0x3F803F80u, 0x3F803F80u};
      if (L == 0) asm volatile("" : "+v"(ones));
      u64 lomask = 0x00ff00ff00ff00ffull;
      asm volatile("" : "+s"(lomask));
      v4u W[16][4];
#pragma unroll
      for (int nb = 0; nb < 4; ++nb) W[0][nb] = W1F(nb);
#define SHUF(ks, n) do { const int e_ = (n) & 1, i_ = ((n) >> 1) & 1; unsigned t_; \
        if ((n) == 4) { asm volatile("s_not_b64 vcc, vcc\n\tv_cndmask_b32_dpp %0, %1, %2, vcc row_ror:8 row_mask:0xf bank_mask:0xf" : "=v"(t_) \
                         : "v"(raw[ks][i_][e_]), "v"(raw[ks][i_][2 + e_]) : "vcc", "scc"); F[ks][e_][2 * i_ + 1] = t_; } else \
        if ((n) < 4) { asm volatile("v_cndmask_b32_dpp %0, %1, %2, vcc row_ror:8 row_mask:0xf bank_mask:0xf" : "=v"(t_) \
                         : "v"(raw[ks][i_][2 + e_]), "v"(raw[ks][i_][e_]) : "vcc"); F[ks][e_][2 * i_] = t_; } \
        else { asm volatile("v_cndmask_b32_dpp %0, %1, %2, vcc row_ror:8 row_mask:0xf bank_mask:0xf" : "=v"(t_) \
                         : "v"(raw[ks][i_][e_]), "v"(raw[ks][i_][2 + e_]) : "vcc"); F[ks][e_][2 * i_ + 1] = t_; } } while (0)
      if (L == 0) {
        asm volatile("s_mov_b64 vcc, %0" :: "s"(lomask) : "vcc");
#pragma unroll
        for (int n = 0; n < 8; ++n) SHUF(0, n);
      }
      MFMA_BEGIN();
      static_for<0, 16>([&](auto kc) __attribute__((always_inline)) {
        constexpr int ks = decltype(kc)::value;
        if constexpr (L == 1) { F[ks][0] = raw[ks][0]; F[ks][1] = raw[ks][1]; }
        if constexpr (L == 1) asm volatile("" :: "v"(W[ks][0]), "v"(W[ks][1]), "v"(F[ks][0]), "v"(F[ks][1]));
        else if constexpr (ks < 15)
          asm volatile("s_mov_b64 vcc, %0" :: "s"(lomask), "v"(W[ks][0]), "v"(W[ks][1]), "v"(raw[ks + 1][0]), "v"(raw[ks + 1][1]) : "vcc");
        else asm volatile("" :: "v"(W[ks][0]), "v"(W[ks][1]));
        SB();
        static_for<0, (L == 0 ? 12 : 8)>([&](auto nc) __attribute__((always_inline)) {
          constexpr int n = decltype(nc)::value, e = n & 1, nb = n >> 1;
          if constexpr (n == 4) { asm volatile("" :: "v"(W[ks][2]), "v"(W[ks][3])); SB(); }
          if constexpr (n < 8) { if constexpr (ks == 0) MF0(a1[nb][e], W[ks][nb], F[ks][e]); else MF(a1[nb][e], W[ks][nb], F[ks][e]); }
          else if constexpr (n < 10) { if constexpr (ks == 0) MF0(aS[e], ones, F[ks][e]); else MF(aS[e], ones, F[ks][e]); }
          else { if constexpr (ks == 0) MF0(aQ[e], F[ks][e], F[ks][e]); else MF(aQ[e], F[ks][e], F[ks][e]); }
          if constexpr (ks < 15 && n < 8) {
            const v2u t = w1h(ks + 1, nb, e);
            W[ks + 1][nb][2 * e] = t.x;
            W[ks + 1][nb][2 * e + 1] = t.y;
            SB();
          }
          if constexpr (ks == 15 && n < 4) { if constexpr (e == 0) bq[0][nb] = BV(nb); else cq[0][nb] = CV(nb); SB(); }
          if constexpr (L == 0 && ks < 15 && n < 8) SHUF(ks + 1, n);
          constexpr int nx = L == 0 ? 8 : 2;
          if constexpr (ks + WIN1 < 16 && (n == nx || n == nx + 1)) raw[ks + WIN1][n - nx] = LD128S(vl16, OX, (ks + WIN1) * 2048 + (n - nx) * 1024);
          if constexpr (L == 0 && ks == 15 && (n == 0 || n == 2)) FILL128(vl16, O_X3 + vbo * X3S + (4 * cs + eo) * 1024, n * 1024);
          if constexpr (L == 0 && ks == 15 && n == 4)
            ST64S((v2u{~0u, ~0u}), lane < 16 ? vl16 : O_END, O_ST3 + (vbo * 2 + eo) * 1024 + (cs >> 1) * 256 + (cs & 1) * 8, 0);
          if constexpr (ks >= 12 && n == 7) FILL_H(OH, ks - 12);
          if constexpr (L == 1 && ks == 10 && (n == 2 || n == 3)) st3[n - 2] = LD128S(vl16, O_ST3 + vb * 2048, (n - 2) * 1024);
          if constexpr (L == 1 && n >= 4) vm = umax3(vm, F[ks][e][n & 2], F[ks][e][(n & 2) + 1]);
          if constexpr (ks == (L == 0 ? 14 : 13) && n >= (L == 0 ? 8 : 4) && n < (L == 0 ? 12 : 8)) {
            constexpr int i = n & 1;
            if constexpr (n & 2) *(LAS unsigned*)(bcw + 256 * i) = b1v[i] << 16; else *(LAS float*)(bcw + 512 + 256 * i) = csv[i];
          }
          SB();
        });
      });
      MFMA_END();
      if (L == 1) vm = umax3(umax3(vm, st3[0].x, st3[0].y), umax3(st3[0].z, st3[0].w, st3[1].x), umax3(st3[1].y, st3[1].z, st3[1].w));
      if (L == 0 || __builtin_expect(!__any(vm == ~0u), 1)) break;
    }
    STAMP(12 + 10 * L);
    bq[1][0] = BV(2); cq[1][0] = CV(2); bq[1][1] = BV(3); cq[1][1] = CV(3);
#pragma unroll
    for (int e = 0; e < 2; ++e) {
      float sm, sq;
      if (L == 0) {
        const float d = (lane & 2) ? ((lane & 1) ? aQ[e][3] : aQ[e][2]) : ((lane & 1) ? aQ[e][1] : aQ[e][0]);
        sq = __uint_as_float(__builtin_amdgcn_ds_bpermute((l15 + 16 * (l15 >> 2)) * 4, __float_as_uint(d)));
        sm = aS[e][0];
      } else {
        sm = sum32x(sum16x(__uint_as_float(st3[e].x) + __uint_as_float(st3[e].z)));
        sq = sum32x(sum16x(__uint_as_float(st3[e].y) + __uint_as_float(st3[e].w)));
      }
      const float mu = sm * (1.0f / 512.0f);
      const float var = __builtin_fmaxf(__builtin_fmaf(-mu, mu, sq * (1.0f / 512.0f)), 0.f);
      const float r1 = __builtin_amdgcn_rsqf(var + EPS);
      const float r = r1 * __builtin_amdgcn_rsqf(__builtin_fmaf(var * r1, r1, EPS));
      rr[e] = r;
      mr[e] = -mu * r;
    }
    SB();
    static_for<0, 4>([&](auto gc) __attribute__((always_inline)) {
      constexpr int gi = decltype(gc)::value, i = gi >> 1, e = gi & 1;
      const f32x2 ac[4] = {lo2(a1[2 * i][e]), hi2(a1[2 * i][e]), lo2(a1[2 * i + 1][e]), hi2(a1[2 * i + 1][e])};
      const f32x2 bb[4] = {lo2(bq[i][0]), hi2(bq[i][0]), lo2(bq[i][1]), hi2(bq[i][1])};
      const f32x2 cc[4] = {lo2(cq[i][0]), hi2(cq[i][0]), lo2(cq[i][1]), hi2(cq[i][1])};
      unsigned o[4];
      gelu_pairs<4, false>(ac, bb, cc, rr[e], mr[e], k1, k3, o, [&](auto hc) __attribute__((always_inline)) {
        constexpr int hp_ = 3 * gi + decltype(hc)::value, x0 = W2A + hp_ * W2E / 12, x1 = W2A + (hp_ + 1) * W2E / 12;
        if constexpr (L == 0) static_for<x0, x1>([&](auto xc) __attribute__((always_inline)) {
          constexpr int x = decltype(xc)::value;
          w2r[x >> 2][x & 3] = LD128S(vl16, sw2, x * 1024);
          SB();
        });
      });
      if constexpr (gi == 0) CODE_PF(pre);
      ST128S((v4u{o[0], o[1], o[2], o[3]}), vl16, OH + hbo + 2 * jq * 1024, e * HT + i * 1024);
      SB();
    });
    STAMP(13 + 10 * L);
    f32x4 a2[3][2];
    f32x4 bp[3], cpv[3];
    v4u rs_[2][4];
    gu64* fh = (gu64*)(ws + O_FH + ((L * 64 + 8 * g + 2 * rq + ((lane >> 2) & 1)) * 16 + 4 * w + (lane & 3)) * 64);
    u64 fv;
    {
      v4u W[16][3];
#pragma unroll
      for (int ks = 0; ks < 2; ++ks)
#pragma unroll
        for (int nb = 0; nb < 3; ++nb) W[ks][nb] = W1F(ks * 7 + 4 + nb);
      MFMA_BEGIN();
      static_for<0, 16>([&](auto kc) __attribute__((always_inline)) {
        constexpr int ks = decltype(kc)::value;
        asm volatile("" :: "v"(W[ks][0]), "v"(W[ks][1]), "v"(W[ks][2]));
        SB();
        static_for<0, 6>([&](auto nc) __attribute__((always_inline)) {
          constexpr int n = decltype(nc)::value, e = n & 1, nb = n >> 1;
          if constexpr (ks == 0) MF0(a2[nb][e], W[ks][nb], F[ks][e]); else MF(a2[nb][e], W[ks][nb], F[ks][e]);
          if constexpr (ks < 14) {
            const v2u t = w1h(ks + 2, 4 + nb, e);
            W[ks + 2][nb][2 * e] = t.x;
            W[ks + 2][nb][2 * e + 1] = t.y;
          }
          if constexpr (ks == 14 && e == 0) bp[nb] = BV(4 + nb);
          if constexpr (ks == 15 && e == 0) cpv[nb] = CV(4 + nb);
          if constexpr (L == 0 && (n == 1 || n == 4) && w2p(ks) + (n == 4) < w2p(ks + 1)) {
            constexpr int i = W2A + W2E + w2p(ks) + (n == 4);
            w2r[i >> 2][i & 3] = LD128S(vl16, sw2, i * 1024);
          }
          if constexpr (ks == DR1 && n == 5) {
            if constexpr (L == 0)
              asm volatile("s_waitcnt vmcnt(%0)" :: "i"(w2p(DR1 + 1)) : "memory");
            else DRAIN();
            asm volatile("" :: "v"(pre));
            POST(O_FH + ((L * 64 + vb) * 16 + jc) * 64);
          }
          SB();
        });
      });
      MFMA_END();
    }
    STAMP(14 + 10 * L);
    int hb[4];
#pragma unroll
    for (int u = 0; u < 4; ++u) hb[u] = OH + (16 * g + 4 * rq + ((w + u) & 3)) * HT + w * HQ;
    v4u hf[14][4];
#define LOAD_H(ks, u) do { if ((ks) < 12) hf[ks][u] = LD128S(vl16, hb[u], (ks) * 1024); \
        else { const v2u x0 = LD64S(vl8, hb[u], (ks) * 1024), x1 = LD64S(vl8, hb[u], (ks) * 1024 + 512); hf[ks][u] = v4u{x0.x, x0.y, x1.x, x1.y}; } } while (0)
    static_for<0, 2>([&](auto ec) __attribute__((always_inline)) {
      constexpr int e = decltype(ec)::value;
      if constexpr (e == 0) { fv = __hip_atomic_load(fh + e, RLX_AGENT); SB(); }
      if constexpr (e == 1) {
        u64 f = fv;
        asm volatile("" : "+v"(f));
        if (__builtin_expect(!__all(f == tag), 0)) wait_flags(fh, tag);
        CFENCE();
#pragma unroll
        for (int ks = 0; ks < WIN2; ++ks)
#pragma unroll
          for (int u = 0; u < 4; ++u) { LOAD_H(ks, u); SB(); }
      }
      const f32x2 ac[6] = {lo2(a2[0][e]), hi2(a2[0][e]), lo2(a2[1][e]), hi2(a2[1][e]), lo2(a2[2][e]), hi2(a2[2][e])};
      const f32x2 bb[6] = {lo2(bp[0]), hi2(bp[0]), lo2(bp[1]), hi2(bp[1]), lo2(bp[2]), hi2(bp[2])};
      const f32x2 cc[6] = {lo2(cpv[0]), hi2(cpv[0]), lo2(cpv[1]), hi2(cpv[1]), lo2(cpv[2]), hi2(cpv[2])};
      unsigned o[6];
      gelu_pairs<6, true>(ac, bb, cc, rr[e], mr[e], k1, k3, o, [](auto) {});
      STAMP(50 + e + 2 * L);
      ST128S((v4u{o[0], o[1], o[2], o[3]}), vl16, OH + hbo + (8 + jq) * 1024, e * HT);
      ST64S((v2u{o[4], o[5]}), vl8, OH + hbo + (12 + (jq >> 1)) * 1024 + (jq & 1) * 512, e * HT);
      SB();
    });
    STAMP(15 + 10 * L);
    f32x4 acc[4][4];
#define RED_W(u, nb) (*(LAS f32x4*)(lds + L_SCR + (((w + (u)) & 3) * 3 + ((u) - 1)) * 4096 + (nb) * 1024 + vl16) = acc[u][nb])
    {
      unsigned vm = 0;
      MFMA_BEGIN();
      static_for<0, 14>([&](auto kc) __attribute__((always_inline)) {
        constexpr int ks = decltype(kc)::value;
        if constexpr (ks >= 8) {
          if (__builtin_expect(__any(vm == ~0u), 0)) {
#pragma unroll 1
            for (unsigned it = 0; it < SPIN_CAP; ++it) {
              CFENCE();
              vm = 0;
#pragma unroll
              for (int u = 0; u < 4; ++u) { LOAD_H(ks, u); vm = umax3(umax3(vm, hf[ks][u].x, hf[ks][u].y), hf[ks][u].z, hf[ks][u].w); }
              if (!__any(vm == ~0u)) break;
            }
            __builtin_amdgcn_s_waitcnt(0x0F70);
            MFMA_BEGIN();
          }
          vm = 0;
        }
        static_for<0, 16>([&](auto nc) __attribute__((always_inline)) {
          constexpr int n = decltype(nc)::value, u = ks == 13 ? ((n >> 2) + 1) & 3 : n >> 2, nb = n & 3;
          if constexpr (ks == 13 && n >= 9) RED_W(1 + (n - 9) / 4, (n - 9) & 3);
          if constexpr (ks == 0) MFA0(acc[u][nb], w2r[ks][nb], hf[ks][u]); else MFA(acc[u][nb], w2r[ks][nb], hf[ks][u]);
          if constexpr (nb == 0 && ks + WIN2 < 14) LOAD_H(ks + WIN2, u);
          if constexpr (L == 0 && ks >= RS0 && ks < RS0 + 2 && nb == 2)
            rs_[ks - RS0][u] = LD128S(vl16, (ks == RS0 ? O_X1 : O_Y) + vbo * XS + cs * 4096 + eo * 4, u * 1024);
          if constexpr (ks >= 7 && ks < 13 && n >= 12) vm = umax3(umax3(vm, hf[ks + 1][nb].x, hf[ks + 1][nb].y), hf[ks + 1][nb].z, hf[ks + 1][nb].w);
          SB();
        });
      });
      MFMA_END();
    }
    STAMP(17 + 10 * L);
#pragma unroll
    for (int x = 7; x < 12; ++x) RED_W(1 + x / 4, x & 3);
    SB();
    if (L == 0) {
      const bool hi = lane & 8;
#pragma unroll
      for (int i = 0; i < 4; ++i) {
        const unsigned xa = rs_[0][i].x, xc = rs_[0][i].z, ya = rs_[1][i].x, yc = rs_[1][i].z;
        const unsigned xs = __builtin_amdgcn_update_dpp(0u, hi ? xa : xc, 0x128, 0xf, 0xf, true);
        const unsigned ys = __builtin_amdgcn_update_dpp(0u, hi ? ya : yc, 0x128, 0xf, 0xf, true);
        xres[2 * i] = hi ? xs : xa; xres[2 * i + 1] = hi ? xc : xs;
        yres[2 * i] = hi ? ys : ya; yres[2 * i + 1] = hi ? yc : ys;
      }
      asm volatile("" : "+v"(xres[0]), "+v"(xres[1]), "+v"(xres[2]), "+v"(xres[3]), "+v"(xres[4]), "+v"(xres[5]), "+v"(xres[6]), "+v"(xres[7]));
      asm volatile("" : "+v"(yres[0]), "+v"(yres[1]), "+v"(yres[2]), "+v"(yres[3]), "+v"(yres[4]), "+v"(yres[5]), "+v"(yres[6]), "+v"(yres[7]));
    }
    f32x2 xo[8];
#pragma unroll
    for (int i = 0; i < 8; ++i) {
      const unsigned bb = b2r[i >> 1][i & 1];
      const f32x4 a = acc[0][i >> 1];
      xo[i] = ((i & 1) ? hi2(a) : lo2(a)) + f32x2{lo16(bb), hi16(bb)} + f32x2{lo16(xres[i]), hi16(xres[i])};
    }
    BAR();
#pragma unroll
    for (int nb = 0; nb < 4; ++nb) {
      const LAS unsigned char* rp = lds + L_SCR + w * 12288 + nb * 1024 + vl16;
      const f32x4 s = (*(const LAS f32x4*)rp + *(const LAS f32x4*)(rp + 4096)) + *(const LAS f32x4*)(rp + 8192);
      xo[2 * nb] += lo2(s);
      xo[2 * nb + 1] += hi2(s);
    }
    STAMP(18 + 10 * L);
    if (L == 0) {
      f32x2 s2 = {0.f, 0.f};
#pragma unroll
      for (int i = 0; i < 8; ++i) {
        const unsigned t = pk(xo[i].x, xo[i].y);
        xo[i] = f32x2{lo16(t), hi16(t)};
        s2 += xo[i];
      }
      const float mb = sum32x(sum16x(s2.x + s2.y)) * (1.0f / 64.0f);
      f32x2 q2 = {0.f, 0.f};
#pragma unroll
      for (int i = 0; i < 8; ++i) { const f32x2 t = xo[i] - mb; q2 = __builtin_elementwise_fma(t, t, q2); }
      const float m2 = sum32x(sum16x(q2.x + q2.y));
      const int so = O_ST2 + (vbo * 2 + eo) * 2048;
      if (lane < 16) ST128S((v4u{__float_as_uint(mb), __float_as_uint(m2), (unsigned)tag, (unsigned)(tag >> 32)}), vl16, so + cs * 256, 0);
      CODE_PF(pre);
      BAR();
      __builtin_amdgcn_s_sleep(SLP_ST);
      v4u s0, s1;
#pragma unroll 1
      for (unsigned it = 0; it < SPIN_CAP; ++it) {
        CFENCE();
        s0 = LD128S(vl16, so, 0);
        s1 = LD128S(vl16, so, 1024);
        if (__all(s0.z == (unsigned)tag && s0.w == (unsigned)(tag >> 32) && s1.z == (unsigned)tag && s1.w == (unsigned)(tag >> 32))) break;
      }
      asm volatile("" :: "v"(pre));
      STAMP(19);
      const float ma = __uint_as_float(s0.x), mc = __uint_as_float(s1.x);
      const float mu = sum32x(sum16x(ma + mc)) * 0.125f;
      const float da = ma - mu, dc = mc - mu;
      const float tot = sum32x(sum16x(__uint_as_float(s0.y) + __uint_as_float(s1.y) + 64.0f * (da * da + dc * dc)));
      const float r = __builtin_amdgcn_rsqf(tot * (1.0f / 512.0f) + EPS);
#pragma unroll
      for (int i = 0; i < 8; ++i) {
        const f32x2 x = __builtin_elementwise_fma((xo[i] - mu) * r, f32x2{lo16(yres[i]), hi16(yres[i])}, xo[i]);
        xres[i] = san(pk(x.x, x.y));
      }
#pragma unroll
      for (int i = 0; i < 2; ++i)
        ST128S((v4u{xres[4 * i], xres[4 * i + 1], xres[4 * i + 2], xres[4 * i + 3]}), vl16, O_X3 + vbo * X3S + (4 * cs + eo) * 1024, i * 2048);
      POST(O_FX + ((vbo * 2 + eo) * 8 + cs) * 64);
      SB();
      STAMP(55);
      s2 = q2 = f32x2{0.f, 0.f};
#pragma unroll
      for (int i = 0; i < 8; ++i) {
        const f32x2 x = {lo16(xres[i]), hi16(xres[i])};
        s2 += x;
        q2 = __builtin_elementwise_fma(x, x, q2);
      }
      const float s3 = sum32x(sum16x(s2.x + s2.y)), q3 = sum32x(sum16x(q2.x + q2.y));
      if (lane < 16) ST64S((v2u{san(__float_as_uint(s3)), san(__float_as_uint(q3))}), vl16, O_ST3 + (vbo * 2 + eo) * 1024 + (cs >> 1) * 256 + (cs & 1) * 8, 0);
      STAMP(20);
      __builtin_amdgcn_s_sleep(SLP_X3);
      wait_flags((gu64*)(ws + O_FX + (vb * 16 + l15) * 64), tag);
      STAMP(21);
    } else {
      const int t = 64 * (vbo & 31) + 8 * (l15 & 7) + 4 * (vbo >> 5) + 2 * (l15 >> 3) + eo;
      unsigned char* op = out + t * 1024 + cs * 128 + (g4 & 1) * 32 + (g4 >> 1) * 16;
#pragma unroll
      for (int i = 0; i < 2; ++i) {
        const auto r0 = __builtin_amdgcn_permlane16_swap(pk(xo[4 * i].x, xo[4 * i].y), pk(xo[4 * i + 2].x, xo[4 * i + 2].y), false, false);
        const auto r1 = __builtin_amdgcn_permlane16_swap(pk(xo[4 * i + 1].x, xo[4 * i + 1].y), pk(xo[4 * i + 3].x, xo[4 * i + 3].y), false, false);
        __builtin_nontemporal_store(v4u{r0[0], r1[0], r0[1], r1[1]}, (v4u*)(op + i * 64));
      }
      STAMP(29);
    }
  };
  layer(IC<0>{});
  layer(IC<1>{});
  STAMP(30);
  asm volatile(".Lh3_end:");
}

extern "C" void kernel_launch(void* const* d_in, const int* in_sizes, int n_in, void* d_out, int out_size, void* d_ws,
                              size_t ws_size, hipStream_t stream) {
  (void)in_sizes; (void)n_in; (void)out_size; (void)ws_size;
  (void)hipFuncSetAttribute((const void*)h3_mixer_kernel, hipFuncAttributeMaxDynamicSharedMemorySize, L_BYTES);
  h3_mixer_kernel<<<256, 256, L_BYTES, stream>>>(
      (const unsigned*)d_in[0], (const unsigned*)d_in[1], (const unsigned*)d_in[2], (const v4u*)d_in[3], (const v4u*)d_in[4],
      (const unsigned*)d_in[5], (const unsigned char*)d_in[7], (const u16*)d_in[8], (const unsigned char*)d_in[9], (const v2u*)d_in[10],
      (unsigned char*)d_out, (unsigned char*)d_ws);
}
```
